```python
import torch

def _softmax(x, axis=-1):
    return torch.softmax(x, dim=axis)
def _gelu(x):
    return torch.nn.functional.gelu(x, approximate="tanh")
def _silu(x):
    return torch.nn.functional.silu(x)
def _sigmoid(x):
    return torch.sigmoid(x)
def _layernorm(x, axis=-1, eps=1e-5):
    m = x.mean(dim=axis, keepdim=True)
    v = x.var(dim=axis, keepdim=True, unbiased=False)
    return (x - m) / torch.sqrt(v + eps)
def _scatter_add(tgt, idx, src, axis=0):
    return tgt.clone().index_add_(axis, idx, src)
def _segment_sum(src, seg, n):
    out = torch.zeros((n,) + src.shape[1:], dtype=src.dtype)
    return out.index_add_(0, seg, src)
def _pad(x, pw):
    flat = tuple(v for pair in reversed(pw) for v in pair)
    return torch.nn.functional.pad(x, flat)

kernel_name = 'recurrent_scan__softmax_full_mha__dense_activation_squared_relu__prompt'

def setup_inputs():

    _w = (lambda a: (lambda t: t.to(torch.bfloat16) if t.is_floating_point() else t)(torch.as_tensor(a)))
    def _A_setup():
        B, S, D = 1, 2048, 640
        return {
            "x": _w((lambda *s: torch.randn(*s, dtype=torch.bfloat16) * 0.05)(B, S, D)),
            "wqkv": _w((lambda *s: torch.randn(*s, dtype=torch.bfloat16) * 0.05)(D, 3 * D)),
            "wo": _w((lambda *s: torch.randn(*s, dtype=torch.bfloat16) * 0.05)(D, D)),
        }
    _A_in = _A_setup()

    def _B_setup():
        B, S, D, F = 1, 2048, 640, 1536
        return {
            "x": _w((lambda *s: torch.randn(*s, dtype=torch.bfloat16) * 0.05)(B, S, D)),
            "w1": _w((lambda *s: torch.randn(*s, dtype=torch.bfloat16) * 0.05)(D, F)),
            "b1": _w((lambda *s: torch.randn(*s, dtype=torch.bfloat16) * 0.8)(F)),
            "w2": _w((lambda *s: torch.randn(*s, dtype=torch.bfloat16) * 0.05)(F, D)),
            "b2": _w((lambda *s: torch.randn(*s, dtype=torch.bfloat16) * 3.2)(D)),
        }
    _B_in = _B_setup()

    return {
        "A_x": _A_in["x"],
        "A_wqkv": _A_in["wqkv"],
        "A_wo": _A_in["wo"],
        "B_x": _B_in["x"],
        "B_w1": _B_in["w1"],
        "B_b1": _B_in["b1"],
        "B_w2": _B_in["w2"],
        "B_b2": _B_in["b2"],
    }

def reference(A_x, A_wqkv, A_wo, B_x, B_w1, B_b1, B_w2, B_b2):
    def _A_ref(x, wqkv, wo):
        B, S, D = x.shape
        H = 32; Dh = D // H
        qkv = (lambda x, *d: x.permute(*d))((x @ wqkv).reshape(B, S, 3, H, Dh), 2, 0, 3, 1, 4)
        q, k, v = qkv[0], qkv[1], qkv[2]
        scale = Dh ** -0.5
        scores = torch.einsum("bhqd,bhkd->bhqk", q, k) * scale
        p = _softmax(scores, axis=-1)
        o = torch.einsum("bhqk,bhkd->bhqd", p, v)
        return (lambda x, *d: x.permute(*d))(o, 0, 2, 1, 3).reshape(B, S, D) @ wo

    def _B_ref(x, w1, b1, w2, b2):
        h = torch.einsum("bsd,df->bsf", x, w1) + b1
        zero = torch.zeros_like(h)
        r = torch.where(h > zero, h, zero)
        h = r * r
        out = torch.einsum("bsf,fd->bsd", h, w2) + b2
        return out

    x = A_x
    xn = _layernorm(x, axis=-1)
    s = _A_ref(xn, A_wqkv, A_wo)
    g = _silu(_B_ref(xn, B_w1, B_b1, B_w2, B_b2))
    return x + s * g
```

```cpp
#include <hip/hip_runtime.h>
#include <hip/hip_bf16.h>
#include <stdint.h>
typedef unsigned short u16;
static constexpr int S = 2048, D = 640, F = 1536, H = 32, DH = 20;
__device__ inline float b2f(u16 v) { return __uint_as_float(((unsigned)v) << 16); }
__device__ inline u16 f2b(float f) { __hip_bfloat16 b = __float2bfloat16(f); return *(u16*)&b; }

__global__ void k_ln(const u16* x, float* xn) {
  int r = blockIdx.x; int l = threadIdx.x;
  float v[10]; float s = 0;
  for (int i = 0; i < 10; i++) { v[i] = b2f(x[r * D + l + 64 * i]); s += v[i]; }
  for (int o = 32; o; o >>= 1) s += __shfl_xor(s, o);
  float m = s / D, q = 0;
  for (int i = 0; i < 10; i++) q += (v[i] - m) * (v[i] - m);
  for (int o = 32; o; o >>= 1) q += __shfl_xor(q, o);
  float rs = rsqrtf(q / D + 1e-5f);
  for (int i = 0; i < 10; i++) xn[r * D + l + 64 * i] = (v[i] - m) * rs;
}
__global__ void k_gemm(const float* A, const u16* W, const u16* bias, float* C, int N, int K, int mode) {
  int n = blockIdx.x * 64 + threadIdx.x; int m0 = blockIdx.y * 8;
  float acc[8] = {};
  for (int k = 0; k < K; k++) { float w = b2f(W[k * N + n]); for (int i = 0; i < 8; i++) acc[i] += A[(m0 + i) * K + k] * w; }
  for (int i = 0; i < 8; i++) {
    float v = acc[i];
    if (mode) v += b2f(bias[n]);
    if (mode == 1) { v = v > 0 ? v : 0; v = v * v; }
    C[(m0 + i) * N + n] = b2f(f2b(v));
  }
}
__global__ void k_attn(const float* qkv, float* o) {
  int h = blockIdx.y; int q = blockIdx.x * 64 + threadIdx.x;
  float qv[DH], acc[DH];
  for (int d = 0; d < DH; d++) { qv[d] = qkv[q * 3 * D + h * DH + d] * 0.22360679775f; acc[d] = 0; }
  float m = -INFINITY, l = 0;
  for (int k = 0; k < S; k++) {
    float s = 0;
    for (int d = 0; d < DH; d++) s += qv[d] * qkv[k * 3 * D + D + h * DH + d];
    float mn = fmaxf(m, s); float a = __expf(m - mn), p = __expf(s - mn);
    l = l * a + p;
    for (int d = 0; d < DH; d++) acc[d] = acc[d] * a + p * qkv[k * 3 * D + 2 * D + h * DH + d];
    m = mn;
  }
  for (int d = 0; d < DH; d++) o[q * D + h * DH + d] = b2f(f2b(acc[d] / l));
}
__global__ void k_fin(const u16* x, const float* s, const float* g, u16* out) {
  int i = blockIdx.x * 256 + threadIdx.x;
  float z = g[i]; float sg = z / (1.f + __expf(-z));
  out[i] = f2b(b2f(x[i]) + s[i] * sg);
}
extern "C" void kernel_launch(void* const* d_in, const int* in_sizes, int n_in, void* d_out, int out_size, void* d_ws, size_t ws_size, hipStream_t stream) {
  const u16* x = (const u16*)d_in[0]; const u16* wqkv = (const u16*)d_in[1]; const u16* wo = (const u16*)d_in[2];
  const u16* w1 = (const u16*)d_in[4]; const u16* b1 = (const u16*)d_in[5]; const u16* w2 = (const u16*)d_in[6]; const u16* b2 = (const u16*)d_in[7];
  float* ws = (float*)d_ws;
  float* xn = ws; float* qkv = xn + S * D; float* hh = qkv + S * 3 * D; float* o = hh + S * F; float* s = o + S * D; float* g = s + S * D;
  k_ln<<<S, 64, 0, stream>>>(x, xn);
  k_gemm<<<dim3(3 * D / 64, S / 8), 64, 0, stream>>>(xn, wqkv, nullptr, qkv, 3 * D, D, 0);
  k_gemm<<<dim3(F / 64, S / 8), 64, 0, stream>>>(xn, w1, b1, hh, F, D, 1);
  k_attn<<<dim3(S / 64, H), 64, 0, stream>>>(qkv, o);
  k_gemm<<<dim3(D / 64, S / 8), 64, 0, stream>>>(o, wo, nullptr, s, D, D, 0);
  k_gemm<<<dim3(D / 64, S / 8), 64, 0, stream>>>(hh, w2, b2, g, D, F, 2);
  k_fin<<<S * D / 256, 256, 0, stream>>>(x, s, g, (u16*)d_out);
}
```

```cpp
#include <hip/hip_runtime.h>
#include <stdint.h>

typedef unsigned short u16;
typedef unsigned long long u64;
#define VEC(T, N) T __attribute__((ext_vector_type(N)))
typedef VEC(float, 16) f32x16;
typedef VEC(unsigned, 2) u32x2;
typedef VEC(unsigned, 4) u32x4;
typedef VEC(__bf16, 2) bf16x2;
typedef VEC(__bf16, 8) bf16x8;
typedef VEC(short, 4) s16x4;
#define DEV __device__ __forceinline__

constexpr unsigned FS = 128;
constexpr unsigned W_EPOCH = 0;
constexpr unsigned W_CS = 4096;
constexpr unsigned W_STAT = 32768;
constexpr unsigned W_FQKV = 65536;
constexpr unsigned W_FH = 196608;
constexpr unsigned W_FO = 327680;
constexpr unsigned W_FP = 458752;
constexpr unsigned W_Q = 589824;
constexpr unsigned W_KV = 3211264;
constexpr unsigned W_H = 8454144;
constexpr unsigned W_O = 14745600;
constexpr unsigned W_P = 17367040;
constexpr unsigned W_END = 20152320;
constexpr int SC1 = 16, SPIN = 1048576;

static __device__ const unsigned lane_tab[6912] __attribute__((aligned(1024))) = {
  0, 64, 65536, 65568, 128, 192, 65544, 65576, 272, 336, 65552, 65584, 400, 464, 65560, 65592,
  576, 512, 65824, 65792, 704, 640, 65832, 65800, 848, 784, 65840, 65808, 976, 912, 65848, 65816,
  1056, 1120, 66112, 66144, 1184, 1248, 66120, 66152, 1328, 1392, 66128, 66160, 1456, 1520, 66136, 66168,
  1632, 1568, 66400, 66368, 1760, 1696, 66408, 66376, 1904, 1840, 66416, 66384, 2032, 1968, 66424, 66392,
  16, 80, 67712, 67744, 144, 208, 67720, 67752, 256, 320, 67728, 67760, 384, 448, 67736, 67768,
  592, 528, 68000, 67968, 720, 656, 68008, 67976, 832, 768, 68016, 67984, 960, 896, 68024, 67992,
  1072, 1136, 68288, 68320, 1200, 1264, 68296, 68328, 1312, 1376, 68304, 68336, 1440, 1504, 68312, 68344,
  1648, 1584, 68576, 68544, 1776, 1712, 68584, 68552, 1888, 1824, 68592, 68560, 2016, 1952, 68600, 68568,
  32, 96, 69632, 69664, 160, 224, 69640, 69672, 304, 368, 69648, 69680, 432, 496, 69656, 69688,
  608, 544, 69920, 69888, 736, 672, 69928, 69896, 880, 816, 69936, 69904, 1008, 944, 69944, 69912,
  1024, 1088, 70208, 70240, 1152, 1216, 70216, 70248, 1296, 1360, 70224, 70256, 1424, 1488, 70232, 70264,
  1600, 1536, 70496, 70464, 1728, 1664, 70504, 70472, 1872, 1808, 70512, 70480, 2000, 1936, 70520, 70488,
  48, 112, 71808, 71840, 176, 240, 71816, 71848, 288, 352, 71824, 71856, 416, 480, 71832, 71864,
  624, 560, 72096, 72064, 752, 688, 72104, 72072, 864, 800, 72112, 72080, 992, 928, 72120, 72088,
  1040, 1104, 72384, 72416, 1168, 1232, 72392, 72424, 1280, 1344, 72400, 72432, 1408, 1472, 72408, 72440,
  1616, 1552, 72672, 72640, 1744, 1680, 72680, 72648, 1856, 1792, 72688, 72656, 1984, 1920, 72696, 72664,
  0, 64, 65536, 65568, 128, 192, 65544, 65576, 272, 336, 65552, 65584, 400, 464, 65560, 65592,
  1056, 1120, 65824, 65792, 1184, 1248, 65832, 65800, 1328, 1392, 65840, 65808, 1456, 1520, 65848, 65816,
  2112, 2048, 66112, 66144, 2240, 2176, 66120, 66152, 2384, 2320, 66128, 66160, 2512, 2448, 66136, 66168,
  3168, 3104, 66400, 66368, 3296, 3232, 66408, 66376, 3440, 3376, 66416, 66384, 3568, 3504, 66424, 66392,
  16, 80, 67712, 67744, 144, 208, 67720, 67752, 256, 320, 67728, 67760, 384, 448, 67736, 67768,
  1072, 1136, 68000, 67968, 1200, 1264, 68008, 67976, 1312, 1376, 68016, 67984, 1440, 1504, 68024, 67992,
  2128, 2064, 68288, 68320, 2256, 2192, 68296, 68328, 2368, 2304, 68304, 68336, 2496, 2432, 68312, 68344,
  3184, 3120, 68576, 68544, 3312, 3248, 68584, 68552, 3424, 3360, 68592, 68560, 3552, 3488, 68600, 68568,
  32, 96, 69632, 69664, 160, 224, 69640, 69672, 304, 368, 69648, 69680, 432, 496, 69656, 69688,
  1024, 1088, 69920, 69888, 1152, 1216, 69928, 69896, 1296, 1360, 69936, 69904, 1424, 1488, 69944, 69912,
  2144, 2080, 70208, 70240, 2272, 2208, 70216, 70248, 2416, 2352, 70224, 70256, 2544, 2480, 70232, 70264,
  3136, 3072, 70496, 70464, 3264, 3200, 70504, 70472, 3408, 3344, 70512, 70480, 3536, 3472, 70520, 70488,
  48, 112, 71808, 71840, 176, 240, 71816, 71848, 288, 352, 71824, 71856, 416, 480, 71832, 71864,
  1040, 1104, 72096, 72064, 1168, 1232, 72104, 72072, 1280, 1344, 72112, 72080, 1408, 1472, 72120, 72088,
  2160, 2096, 72384, 72416, 2288, 2224, 72392, 72424, 2400, 2336, 72400, 72432, 2528, 2464, 72408, 72440,
  3152, 3088, 72672, 72640, 3280, 3216, 72680, 72648, 3392, 3328, 72688, 72656, 3520, 3456, 72696, 72664,
  0, 2048, 156160, 0, 16, 2064, 156164, 0, 32, 2080, 156168, 0, 48, 2096, 156172, 0,
  64, 2112, 156176, 0, 80, 2128, 156180, 0, 96, 2144, 156184, 0, 112, 2160, 156188, 0,
  128, 2176, 156192, 0, 144, 2192, 156196, 0, 160, 2208, 156200, 0, 176, 2224, 156204, 0,
  192, 2240, 156208, 0, 208, 2256, 156212, 0, 224, 2272, 156216, 0, 240, 2288, 156220, 0,
  512, 83968, 156160, 0, 528, 83984, 156164, 0, 544, 84000, 156168, 0, 560, 84016, 156172, 0,
  576, 84032, 156176, 0, 592, 84048, 156180, 0, 608, 84064, 156184, 0, 624, 84080, 156188, 0,
  640, 84096, 156192, 0, 656, 84112, 156196, 0, 672, 84128, 156200, 0, 688, 84144, 156204, 0,
  704, 84160, 156208, 0, 720, 84176, 156212, 0, 736, 84192, 156216, 0, 752, 84208, 156220, 0,
  1024, 82944, 156160, 3, 1040, 82960, 156164, 3, 1056, 82976, 156168, 3, 1072, 82992, 156172, 3,
  1088, 83008, 156176, 3, 1104, 83024, 156180, 3, 1120, 83040, 156184, 3, 1136, 83056, 156188, 3,
  1152, 83072, 156192, 3, 1168, 83088, 156196, 3, 1184, 83104, 156200, 3, 1200, 83120, 156204, 3,
  1216, 83136, 156208, 3, 1232, 83152, 156212, 3, 1248, 83168, 156216, 3, 1264, 83184, 156220, 3,
  1536, 83456, 156160, 3, 1552, 83472, 156164, 3, 1568, 83488, 156168, 3, 1584, 83504, 156172, 3,
  1600, 83520, 156176, 3, 1616, 83536, 156180, 3, 1632, 83552, 156184, 3, 1648, 83568, 156188, 3,
  1664, 83584, 156192, 3, 1680, 83600, 156196, 3, 1696, 83616, 156200, 3, 1712, 83632, 156204, 3,
  1728, 83648, 156208, 3, 1744, 83664, 156212, 3, 1760, 83680, 156216, 3, 1776, 83696, 156220, 3,
  81920, 164864, 156160, 2, 81936, 164880, 156164, 2, 81952, 164896, 156168, 2, 81968, 164912, 156172, 2,
  81984, 164928, 156176, 2, 82000, 164944, 156180, 2, 82016, 164960, 156184, 2, 82032, 164976, 156188, 2,
  82048, 164992, 156192, 2, 82064, 165008, 156196, 2, 82080, 165024, 156200, 2, 82096, 165040, 156204, 2,
  82112, 165056, 156208, 2, 82128, 165072, 156212, 2, 82144, 165088, 156216, 2, 82160, 165104, 156220, 2,
  82432, 165376, 156160, 2, 82448, 165392, 156164, 2, 82464, 165408, 156168, 2, 82480, 165424, 156172, 2,
  82496, 165440, 156176, 2, 82512, 165456, 156180, 2, 82528, 165472, 156184, 2, 82544, 165488, 156188, 2,
  82560, 165504, 156192, 2, 82576, 165520, 156196, 2, 82592, 165536, 156200, 2, 82608, 165552, 156204, 2,
  82624, 165568, 156208, 2, 82640, 165584, 156212, 2, 82656, 165600, 156216, 2, 82672, 165616, 156220, 2,
  163840, 165888, 156160, 0, 163856, 165904, 156164, 0, 163872, 165920, 156168, 0, 163888, 165936, 156172, 0,
  163904, 165952, 156176, 0, 163920, 165968, 156180, 0, 163936, 165984, 156184, 0, 163952, 166000, 156188, 0,
  163968, 166016, 156192, 0, 163984, 166032, 156196, 0, 164000, 166048, 156200, 0, 164016, 166064, 156204, 0,
  164032, 166080, 156208, 0, 164048, 166096, 156212, 0, 164064, 166112, 156216, 0, 164080, 166128, 156220, 0,
  164352, 247808, 156160, 0, 164368, 247824, 156164, 0, 164384, 247840, 156168, 0, 164400, 247856, 156172, 0,
  164416, 247872, 156176, 0, 164432, 247888, 156180, 0, 164448, 247904, 156184, 0, 164464, 247920, 156188, 0,
  164480, 247936, 156192, 0, 164496, 247952, 156196, 0, 164512, 247968, 156200, 0, 164528, 247984, 156204, 0,
  164544, 248000, 156208, 0, 164560, 248016, 156212, 0, 164576, 248032, 156216, 0, 164592, 248048, 156220, 0,
  246784, 327680, 156160, 1, 246800, 327696, 156164, 1, 246816, 327712, 156168, 1, 246832, 327728, 156172, 1,
  246848, 327744, 156176, 1, 246864, 327760, 156180, 1, 246880, 327776, 156184, 1, 246896, 327792, 156188, 1,
  246912, 327808, 156192, 1, 246928, 327824, 156196, 1, 246944, 327840, 156200, 1, 246960, 327856, 156204, 1,
  246976, 327872, 156208, 1, 246992, 327888, 156212, 1, 247008, 327904, 156216, 1, 247024, 327920, 156220, 1,
  247296, 328192, 156160, 1, 247312, 328208, 156164, 1, 247328, 328224, 156168, 1, 247344, 328240, 156172, 1,
  247360, 328256, 156176, 1, 247376, 328272, 156180, 1, 247392, 328288, 156184, 1, 247408, 328304, 156188, 1,
  247424, 328320, 156192, 1, 247440, 328336, 156196, 1, 247456, 328352, 156200, 1, 247472, 328368, 156204, 1,
  247488, 328384, 156208, 1, 247504, 328400, 156212, 1, 247520, 328416, 156216, 1, 247536, 328432, 156220, 1,
  245760, 328704, 156160, 2, 245776, 328720, 156164, 2, 245792, 328736, 156168, 2, 245808, 328752, 156172, 2,
  245824, 328768, 156176, 2, 245840, 328784, 156180, 2, 245856, 328800, 156184, 2, 245872, 328816, 156188, 2,
  245888, 328832, 156192, 2, 245904, 328848, 156196, 2, 245920, 328864, 156200, 2, 245936, 328880, 156204, 2,
  245952, 328896, 156208, 2, 245968, 328912, 156212, 2, 245984, 328928, 156216, 2, 246000, 328944, 156220, 2,
  246272, 329216, 156160, 2, 246288, 329232, 156164, 2, 246304, 329248, 156168, 2, 246320, 329264, 156172, 2,
  246336, 329280, 156176, 2, 246352, 329296, 156180, 2, 246368, 329312, 156184, 2, 246384, 329328, 156188, 2,
  246400, 329344, 156192, 2, 246416, 329360, 156196, 2, 246432, 329376, 156200, 2, 246448, 329392, 156204, 2,
  246464, 329408, 156208, 2, 246480, 329424, 156212, 2, 246496, 329440, 156216, 2, 246512, 329456, 156220, 2,
  329728, 409600, 156160, 0, 329744, 409616, 156164, 0, 329760, 409632, 156168, 0, 329776, 409648, 156172, 0,
  329792, 409664, 156176, 0, 329808, 409680, 156180, 0, 329824, 409696, 156184, 0, 329840, 409712, 156188, 0,
  329856, 409728, 156192, 0, 329872, 409744, 156196, 0, 329888, 409760, 156200, 0, 329904, 409776, 156204, 0,
  329920, 409792, 156208, 0, 329936, 409808, 156212, 0, 329952, 409824, 156216, 0, 329968, 409840, 156220, 0,
  411648, 410112, 156160, 0, 411664, 410128, 156164, 0, 411680, 410144, 156168, 0, 411696, 410160, 156172, 0,
  411712, 410176, 156176, 0, 411728, 410192, 156180, 0, 411744, 410208, 156184, 0, 411760, 410224, 156188, 0,
  411776, 410240, 156192, 0, 411792, 410256, 156196, 0, 411808, 410272, 156200, 0, 411824, 410288, 156204, 0,
  411840, 410304, 156208, 0, 411856, 410320, 156212, 0, 411872, 410336, 156216, 0, 411888, 410352, 156220, 0,
  410624, 491520, 156160, 1, 410640, 491536, 156164, 1, 410656, 491552, 156168, 1, 410672, 491568, 156172, 1,
  410688, 491584, 156176, 1, 410704, 491600, 156180, 1, 410720, 491616, 156184, 1, 410736, 491632, 156188, 1,
  410752, 491648, 156192, 1, 410768, 491664, 156196, 1, 410784, 491680, 156200, 1, 410800, 491696, 156204, 1,
  410816, 491712, 156208, 1, 410832, 491728, 156212, 1, 410848, 491744, 156216, 1, 410864, 491760, 156220, 1,
  411136, 492032, 156160, 1, 411152, 492048, 156164, 1, 411168, 492064, 156168, 1, 411184, 492080, 156172, 1,
  411200, 492096, 156176, 1, 411216, 492112, 156180, 1, 411232, 492128, 156184, 1, 411248, 492144, 156188, 1,
  411264, 492160, 156192, 1, 411280, 492176, 156196, 1, 411296, 492192, 156200, 1, 411312, 492208, 156204, 1,
  411328, 492224, 156208, 1, 411344, 492240, 156212, 1, 411360, 492256, 156216, 1, 411376, 492272, 156220, 1,
  492544, 574464, 156160, 3, 492560, 574480, 156164, 3, 492576, 574496, 156168, 3, 492592, 574512, 156172, 3,
  492608, 574528, 156176, 3, 492624, 574544, 156180, 3, 492640, 574560, 156184, 3, 492656, 574576, 156188, 3,
  492672, 574592, 156192, 3, 492688, 574608, 156196, 3, 492704, 574624, 156200, 3, 492720, 574640, 156204, 3,
  492736, 574656, 156208, 3, 492752, 574672, 156212, 3, 492768, 574688, 156216, 3, 492784, 574704, 156220, 3,
  493056, 574976, 156160, 3, 493072, 574992, 156164, 3, 493088, 575008, 156168, 3, 493104, 575024, 156172, 3,
  493120, 575040, 156176, 3, 493136, 575056, 156180, 3, 493152, 575072, 156184, 3, 493168, 575088, 156188, 3,
  493184, 575104, 156192, 3, 493200, 575120, 156196, 3, 493216, 575136, 156200, 3, 493232, 575152, 156204, 3,
  493248, 575168, 156208, 3, 493264, 575184, 156212, 3, 493280, 575200, 156216, 3, 493296, 575216, 156220, 3,
  493568, 573440, 156160, 0, 493584, 573456, 156164, 0, 493600, 573472, 156168, 0, 493616, 573488, 156172, 0,
  493632, 573504, 156176, 0, 493648, 573520, 156180, 0, 493664, 573536, 156184, 0, 493680, 573552, 156188, 0,
  493696, 573568, 156192, 0, 493712, 573584, 156196, 0, 493728, 573600, 156200, 0, 493744, 573616, 156204, 0,
  493760, 573632, 156208, 0, 493776, 573648, 156212, 0, 493792, 573664, 156216, 0, 493808, 573680, 156220, 0,
  575488, 573952, 156160, 0, 575504, 573968, 156164, 0, 575520, 573984, 156168, 0, 575536, 574000, 156172, 0,
  575552, 574016, 156176, 0, 575568, 574032, 156180, 0, 575584, 574048, 156184, 0, 575600, 574064, 156188, 0,
  575616, 574080, 156192, 0, 575632, 574096, 156196, 0, 575648, 574112, 156200, 0, 575664, 574128, 156204, 0,
  575680, 574144, 156208, 0, 575696, 574160, 156212, 0, 575712, 574176, 156216, 0, 575728, 574192, 156220, 0,
  0, 2048, 156160, 0, 16, 2064, 156164, 0, 32, 2080, 156168, 0, 48, 2096, 156172, 0,
  64, 2112, 156176, 0, 80, 2128, 156180, 0, 96, 2144, 156184, 0, 112, 2160, 156188, 0,
  128, 2176, 156192, 0, 144, 2192, 156196, 0, 160, 2208, 156200, 0, 176, 2224, 156204, 0,
  192, 2240, 156208, 0, 208, 2256, 156212, 0, 224, 2272, 156216, 0, 240, 2288, 156220, 0,
  512, 165888, 156160, 0, 528, 165904, 156164, 0, 544, 165920, 156168, 0, 560, 165936, 156172, 0,
  576, 165952, 156176, 0, 592, 165968, 156180, 0, 608, 165984, 156184, 0, 624, 166000, 156188, 0,
  640, 166016, 156192, 0, 656, 166032, 156196, 0, 672, 166048, 156200, 0, 688, 166064, 156204, 0,
  704, 166080, 156208, 0, 720, 166096, 156212, 0, 736, 166112, 156216, 0, 752, 166128, 156220, 0,
  1024, 164864, 156160, 3, 1040, 164880, 156164, 3, 1056, 164896, 156168, 3, 1072, 164912, 156172, 3,
  1088, 164928, 156176, 3, 1104, 164944, 156180, 3, 1120, 164960, 156184, 3, 1136, 164976, 156188, 3,
  1152, 164992, 156192, 3, 1168, 165008, 156196, 3, 1184, 165024, 156200, 3, 1200, 165040, 156204, 3,
  1216, 165056, 156208, 3, 1232, 165072, 156212, 3, 1248, 165088, 156216, 3, 1264, 165104, 156220, 3,
  1536, 165376, 156160, 3, 1552, 165392, 156164, 3, 1568, 165408, 156168, 3, 1584, 165424, 156172, 3,
  1600, 165440, 156176, 3, 1616, 165456, 156180, 3, 1632, 165472, 156184, 3, 1648, 165488, 156188, 3,
  1664, 165504, 156192, 3, 1680, 165520, 156196, 3, 1696, 165536, 156200, 3, 1712, 165552, 156204, 3,
  1728, 165568, 156208, 3, 1744, 165584, 156212, 3, 1760, 165600, 156216, 3, 1776, 165616, 156220, 3,
  163840, 328704, 156160, 2, 163856, 328720, 156164, 2, 163872, 328736, 156168, 2, 163888, 328752, 156172, 2,
  163904, 328768, 156176, 2, 163920, 328784, 156180, 2, 163936, 328800, 156184, 2, 163952, 328816, 156188, 2,
  163968, 328832, 156192, 2, 163984, 328848, 156196, 2, 164000, 328864, 156200, 2, 164016, 328880, 156204, 2,
  164032, 328896, 156208, 2, 164048, 328912, 156212, 2, 164064, 328928, 156216, 2, 164080, 328944, 156220, 2,
  164352, 329216, 156160, 2, 164368, 329232, 156164, 2, 164384, 329248, 156168, 2, 164400, 329264, 156172, 2,
  164416, 329280, 156176, 2, 164432, 329296, 156180, 2, 164448, 329312, 156184, 2, 164464, 329328, 156188, 2,
  164480, 329344, 156192, 2, 164496, 329360, 156196, 2, 164512, 329376, 156200, 2, 164528, 329392, 156204, 2,
  164544, 329408, 156208, 2, 164560, 329424, 156212, 2, 164576, 329440, 156216, 2, 164592, 329456, 156220, 2,
  327680, 329728, 156160, 0, 327696, 329744, 156164, 0, 327712, 329760, 156168, 0, 327728, 329776, 156172, 0,
  327744, 329792, 156176, 0, 327760, 329808, 156180, 0, 327776, 329824, 156184, 0, 327792, 329840, 156188, 0,
  327808, 329856, 156192, 0, 327824, 329872, 156196, 0, 327840, 329888, 156200, 0, 327856, 329904, 156204, 0,
  327872, 329920, 156208, 0, 327888, 329936, 156212, 0, 327904, 329952, 156216, 0, 327920, 329968, 156220, 0,
  328192, 493568, 156160, 0, 328208, 493584, 156164, 0, 328224, 493600, 156168, 0, 328240, 493616, 156172, 0,
  328256, 493632, 156176, 0, 328272, 493648, 156180, 0, 328288, 493664, 156184, 0, 328304, 493680, 156188, 0,
  328320, 493696, 156192, 0, 328336, 493712, 156196, 0, 328352, 493728, 156200, 0, 328368, 493744, 156204, 0,
  328384, 493760, 156208, 0, 328400, 493776, 156212, 0, 328416, 493792, 156216, 0, 328432, 493808, 156220, 0,
  492544, 655360, 156160, 1, 492560, 655376, 156164, 1, 492576, 655392, 156168, 1, 492592, 655408, 156172, 1,
  492608, 655424, 156176, 1, 492624, 655440, 156180, 1, 492640, 655456, 156184, 1, 492656, 655472, 156188, 1,
  492672, 655488, 156192, 1, 492688, 655504, 156196, 1, 492704, 655520, 156200, 1, 492720, 655536, 156204, 1,
  492736, 655552, 156208, 1, 492752, 655568, 156212, 1, 492768, 655584, 156216, 1, 492784, 655600, 156220, 1,
  493056, 655872, 156160, 1, 493072, 655888, 156164, 1, 493088, 655904, 156168, 1, 493104, 655920, 156172, 1,
  493120, 655936, 156176, 1, 493136, 655952, 156180, 1, 493152, 655968, 156184, 1, 493168, 655984, 156188, 1,
  493184, 656000, 156192, 1, 493200, 656016, 156196, 1, 493216, 656032, 156200, 1, 493232, 656048, 156204, 1,
  493248, 656064, 156208, 1, 493264, 656080, 156212, 1, 493280, 656096, 156216, 1, 493296, 656112, 156220, 1,
  491520, 656384, 156160, 2, 491536, 656400, 156164, 2, 491552, 656416, 156168, 2, 491568, 656432, 156172, 2,
  491584, 656448, 156176, 2, 491600, 656464, 156180, 2, 491616, 656480, 156184, 2, 491632, 656496, 156188, 2,
  491648, 656512, 156192, 2, 491664, 656528, 156196, 2, 491680, 656544, 156200, 2, 491696, 656560, 156204, 2,
  491712, 656576, 156208, 2, 491728, 656592, 156212, 2, 491744, 656608, 156216, 2, 491760, 656624, 156220, 2,
  492032, 656896, 156160, 2, 492048, 656912, 156164, 2, 492064, 656928, 156168, 2, 492080, 656944, 156172, 2,
  492096, 656960, 156176, 2, 492112, 656976, 156180, 2, 492128, 656992, 156184, 2, 492144, 657008, 156188, 2,
  492160, 657024, 156192, 2, 492176, 657040, 156196, 2, 492192, 657056, 156200, 2, 492208, 657072, 156204, 2,
  492224, 657088, 156208, 2, 492240, 657104, 156212, 2, 492256, 657120, 156216, 2, 492272, 657136, 156220, 2,
  657408, 819200, 156160, 0, 657424, 819216, 156164, 0, 657440, 819232, 156168, 0, 657456, 819248, 156172, 0,
  657472, 819264, 156176, 0, 657488, 819280, 156180, 0, 657504, 819296, 156184, 0, 657520, 819312, 156188, 0,
  657536, 819328, 156192, 0, 657552, 819344, 156196, 0, 657568, 819360, 156200, 0, 657584, 819376, 156204, 0,
  657600, 819392, 156208, 0, 657616, 819408, 156212, 0, 657632, 819424, 156216, 0, 657648, 819440, 156220, 0,
  821248, 819712, 156160, 0, 821264, 819728, 156164, 0, 821280, 819744, 156168, 0, 821296, 819760, 156172, 0,
  821312, 819776, 156176, 0, 821328, 819792, 156180, 0, 821344, 819808, 156184, 0, 821360, 819824, 156188, 0,
  821376, 819840, 156192, 0, 821392, 819856, 156196, 0, 821408, 819872, 156200, 0, 821424, 819888, 156204, 0,
  821440, 819904, 156208, 0, 821456, 819920, 156212, 0, 821472, 819936, 156216, 0, 821488, 819952, 156220, 0,
  820224, 983040, 156160, 1, 820240, 983056, 156164, 1, 820256, 983072, 156168, 1, 820272, 983088, 156172, 1,
  820288, 983104, 156176, 1, 820304, 983120, 156180, 1, 820320, 983136, 156184, 1, 820336, 983152, 156188, 1,
  820352, 983168, 156192, 1, 820368, 983184, 156196, 1, 820384, 983200, 156200, 1, 820400, 983216, 156204, 1,
  820416, 983232, 156208, 1, 820432, 983248, 156212, 1, 820448, 983264, 156216, 1, 820464, 983280, 156220, 1,
  820736, 983552, 156160, 1, 820752, 983568, 156164, 1, 820768, 983584, 156168, 1, 820784, 983600, 156172, 1,
  820800, 983616, 156176, 1, 820816, 983632, 156180, 1, 820832, 983648, 156184, 1, 820848, 983664, 156188, 1,
  820864, 983680, 156192, 1, 820880, 983696, 156196, 1, 820896, 983712, 156200, 1, 820912, 983728, 156204, 1,
  820928, 983744, 156208, 1, 820944, 983760, 156212, 1, 820960, 983776, 156216, 1, 820976, 983792, 156220, 1,
  984064, 1147904, 156160, 3, 984080, 1147920, 156164, 3, 984096, 1147936, 156168, 3, 984112, 1147952, 156172, 3,
  984128, 1147968, 156176, 3, 984144, 1147984, 156180, 3, 984160, 1148000, 156184, 3, 984176, 1148016, 156188, 3,
  984192, 1148032, 156192, 3, 984208, 1148048, 156196, 3, 984224, 1148064, 156200, 3, 984240, 1148080, 156204, 3,
  984256, 1148096, 156208, 3, 984272, 1148112, 156212, 3, 984288, 1148128, 156216, 3, 984304, 1148144, 156220, 3,
  984576, 1148416, 156160, 3, 984592, 1148432, 156164, 3, 984608, 1148448, 156168, 3, 984624, 1148464, 156172, 3,
  984640, 1148480, 156176, 3, 984656, 1148496, 156180, 3, 984672, 1148512, 156184, 3, 984688, 1148528, 156188, 3,
  984704, 1148544, 156192, 3, 984720, 1148560, 156196, 3, 984736, 1148576, 156200, 3, 984752, 1148592, 156204, 3,
  984768, 1148608, 156208, 3, 984784, 1148624, 156212, 3, 984800, 1148640, 156216, 3, 984816, 1148656, 156220, 3,
  985088, 1146880, 156160, 0, 985104, 1146896, 156164, 0, 985120, 1146912, 156168, 0, 985136, 1146928, 156172, 0,
  985152, 1146944, 156176, 0, 985168, 1146960, 156180, 0, 985184, 1146976, 156184, 0, 985200, 1146992, 156188, 0,
  985216, 1147008, 156192, 0, 985232, 1147024, 156196, 0, 985248, 1147040, 156200, 0, 985264, 1147056, 156204, 0,
  985280, 1147072, 156208, 0, 985296, 1147088, 156212, 0, 985312, 1147104, 156216, 0, 985328, 1147120, 156220, 0,
  1148928, 1147392, 156160, 0, 1148944, 1147408, 156164, 0, 1148960, 1147424, 156168, 0, 1148976, 1147440, 156172, 0,
  1148992, 1147456, 156176, 0, 1149008, 1147472, 156180, 0, 1149024, 1147488, 156184, 0, 1149040, 1147504, 156188, 0,
  1149056, 1147520, 156192, 0, 1149072, 1147536, 156196, 0, 1149088, 1147552, 156200, 0, 1149104, 1147568, 156204, 0,
  1149120, 1147584, 156208, 0, 1149136, 1147600, 156212, 0, 1149152, 1147616, 156216, 0, 1149168, 1147632, 156220, 0,
  2560, 3072, 156160, 0, 2576, 3088, 156160, 0, 2592, 3104, 156160, 0, 2608, 3120, 156160, 0,
  2688, 166400, 156160, 0, 2704, 166416, 156160, 0, 2720, 166432, 156160, 0, 2736, 166448, 156160, 0,
  2816, 166528, 156160, 0, 2832, 166544, 156160, 0, 2848, 166560, 156160, 0, 2864, 166576, 156160, 0,
  2944, 166656, 156160, 0, 2960, 166672, 156160, 0, 2976, 166688, 156160, 0, 2992, 166704, 156160, 0,
  2624, 3136, 156192, 0, 2640, 3152, 156192, 0, 2656, 3168, 156192, 0, 2672, 3184, 156192, 0,
  2752, 166464, 156192, 0, 2768, 166480, 156192, 0, 2784, 166496, 156192, 0, 2800, 166512, 156192, 0,
  2880, 166592, 156192, 0, 2896, 166608, 156192, 0, 2912, 166624, 156192, 0, 2928, 166640, 156192, 0,
  3008, 166720, 156192, 0, 3024, 166736, 156192, 0, 3040, 166752, 156192, 0, 3056, 166768, 156192, 0,
  3200, 3712, 156224, 3, 3216, 3728, 156224, 3, 3232, 3744, 156224, 3, 3248, 3760, 156224, 3,
  3328, 167040, 156224, 3, 3344, 167056, 156224, 3, 3360, 167072, 156224, 3, 3376, 167088, 156224, 3,
  3456, 167168, 156224, 3, 3472, 167184, 156224, 3, 3488, 167200, 156224, 3, 3504, 167216, 156224, 3,
  3584, 167296, 156224, 3, 3600, 167312, 156224, 3, 3616, 167328, 156224, 3, 3632, 167344, 156224, 3,
  3264, 3776, 156256, 3, 3280, 3792, 156256, 3, 3296, 3808, 156256, 3, 3312, 3824, 156256, 3,
  3392, 167104, 156256, 3, 3408, 167120, 156256, 3, 3424, 167136, 156256, 3, 3440, 167152, 156256, 3,
  3520, 167232, 156256, 3, 3536, 167248, 156256, 3, 3552, 167264, 156256, 3, 3568, 167280, 156256, 3,
  3648, 167360, 156256, 3, 3664, 167376, 156256, 3, 3680, 167392, 156256, 3, 3696, 167408, 156256, 3,
  166784, 330496, 156160, 2, 166800, 330512, 156160, 2, 166816, 330528, 156160, 2, 166832, 330544, 156160, 2,
  166912, 330624, 156160, 2, 166928, 330640, 156160, 2, 166944, 330656, 156160, 2, 166960, 330672, 156160, 2,
  330240, 330752, 156160, 2, 330256, 330768, 156160, 2, 330272, 330784, 156160, 2, 330288, 330800, 156160, 2,
  330368, 494080, 156160, 2, 330384, 494096, 156160, 2, 330400, 494112, 156160, 2, 330416, 494128, 156160, 2,
  166848, 330560, 156192, 2, 166864, 330576, 156192, 2, 166880, 330592, 156192, 2, 166896, 330608, 156192, 2,
  166976, 330688, 156192, 2, 166992, 330704, 156192, 2, 167008, 330720, 156192, 2, 167024, 330736, 156192, 2,
  330304, 330816, 156192, 2, 330320, 330832, 156192, 2, 330336, 330848, 156192, 2, 330352, 330864, 156192, 2,
  330432, 494144, 156192, 2, 330448, 494160, 156192, 2, 330464, 494176, 156192, 2, 330480, 494192, 156192, 2,
  167424, 331136, 156224, 0, 167440, 331152, 156224, 0, 167456, 331168, 156224, 0, 167472, 331184, 156224, 0,
  167552, 331264, 156224, 0, 167568, 331280, 156224, 0, 167584, 331296, 156224, 0, 167600, 331312, 156224, 0,
  330880, 331392, 156224, 0, 330896, 331408, 156224, 0, 330912, 331424, 156224, 0, 330928, 331440, 156224, 0,
  331008, 494720, 156224, 0, 331024, 494736, 156224, 0, 331040, 494752, 156224, 0, 331056, 494768, 156224, 0,
  167488, 331200, 156256, 0, 167504, 331216, 156256, 0, 167520, 331232, 156256, 0, 167536, 331248, 156256, 0,
  167616, 331328, 156256, 0, 167632, 331344, 156256, 0, 167648, 331360, 156256, 0, 167664, 331376, 156256, 0,
  330944, 331456, 156256, 0, 330960, 331472, 156256, 0, 330976, 331488, 156256, 0, 330992, 331504, 156256, 0,
  331072, 494784, 156256, 0, 331088, 494800, 156256, 0, 331104, 494816, 156256, 0, 331120, 494832, 156256, 0,
  494208, 657920, 156160, 1, 494224, 657936, 156160, 1, 494240, 657952, 156160, 1, 494256, 657968, 156160, 1,
  494336, 658048, 156160, 1, 494352, 658064, 156160, 1, 494368, 658080, 156160, 1, 494384, 658096, 156160, 1,
  494464, 658176, 156160, 1, 494480, 658192, 156160, 1, 494496, 658208, 156160, 1, 494512, 658224, 156160, 1,
  494592, 658304, 156160, 1, 494608, 658320, 156160, 1, 494624, 658336, 156160, 1, 494640, 658352, 156160, 1,
  494272, 657984, 156192, 1, 494288, 658000, 156192, 1, 494304, 658016, 156192, 1, 494320, 658032, 156192, 1,
  494400, 658112, 156192, 1, 494416, 658128, 156192, 1, 494432, 658144, 156192, 1, 494448, 658160, 156192, 1,
  494528, 658240, 156192, 1, 494544, 658256, 156192, 1, 494560, 658272, 156192, 1, 494576, 658288, 156192, 1,
  494656, 658368, 156192, 1, 494672, 658384, 156192, 1, 494688, 658400, 156192, 1, 494704, 658416, 156192, 1,
  494848, 658560, 156224, 2, 494864, 658576, 156224, 2, 494880, 658592, 156224, 2, 494896, 658608, 156224, 2,
  494976, 658688, 156224, 2, 494992, 658704, 156224, 2, 495008, 658720, 156224, 2, 495024, 658736, 156224, 2,
  495104, 658816, 156224, 2, 495120, 658832, 156224, 2, 495136, 658848, 156224, 2, 495152, 658864, 156224, 2,
  495232, 658944, 156224, 2, 495248, 658960, 156224, 2, 495264, 658976, 156224, 2, 495280, 658992, 156224, 2,
  494912, 658624, 156256, 2, 494928, 658640, 156256, 2, 494944, 658656, 156256, 2, 494960, 658672, 156256, 2,
  495040, 658752, 156256, 2, 495056, 658768, 156256, 2, 495072, 658784, 156256, 2, 495088, 658800, 156256, 2,
  495168, 658880, 156256, 2, 495184, 658896, 156256, 2, 495200, 658912, 156256, 2, 495216, 658928, 156256, 2,
  495296, 659008, 156256, 2, 495312, 659024, 156256, 2, 495328, 659040, 156256, 2, 495344, 659056, 156256, 2,
  658432, 822144, 156160, 0, 658448, 822160, 156160, 0, 658464, 822176, 156160, 0, 658480, 822192, 156160, 0,
  821760, 822272, 156160, 0, 821776, 822288, 156160, 0, 821792, 822304, 156160, 0, 821808, 822320, 156160, 0,
  821888, 985600, 156160, 0, 821904, 985616, 156160, 0, 821920, 985632, 156160, 0, 821936, 985648, 156160, 0,
  822016, 985728, 156160, 0, 822032, 985744, 156160, 0, 822048, 985760, 156160, 0, 822064, 985776, 156160, 0,
  658496, 822208, 156192, 0, 658512, 822224, 156192, 0, 658528, 822240, 156192, 0, 658544, 822256, 156192, 0,
  821824, 822336, 156192, 0, 821840, 822352, 156192, 0, 821856, 822368, 156192, 0, 821872, 822384, 156192, 0,
  821952, 985664, 156192, 0, 821968, 985680, 156192, 0, 821984, 985696, 156192, 0, 822000, 985712, 156192, 0,
  822080, 985792, 156192, 0, 822096, 985808, 156192, 0, 822112, 985824, 156192, 0, 822128, 985840, 156192, 0,
  659072, 822784, 156224, 1, 659088, 822800, 156224, 1, 659104, 822816, 156224, 1, 659120, 822832, 156224, 1,
  822400, 822912, 156224, 1, 822416, 822928, 156224, 1, 822432, 822944, 156224, 1, 822448, 822960, 156224, 1,
  822528, 986240, 156224, 1, 822544, 986256, 156224, 1, 822560, 986272, 156224, 1, 822576, 986288, 156224, 1,
  822656, 986368, 156224, 1, 822672, 986384, 156224, 1, 822688, 986400, 156224, 1, 822704, 986416, 156224, 1,
  659136, 822848, 156256, 1, 659152, 822864, 156256, 1, 659168, 822880, 156256, 1, 659184, 822896, 156256, 1,
  822464, 822976, 156256, 1, 822480, 822992, 156256, 1, 822496, 823008, 156256, 1, 822512, 823024, 156256, 1,
  822592, 986304, 156256, 1, 822608, 986320, 156256, 1, 822624, 986336, 156256, 1, 822640, 986352, 156256, 1,
  822720, 986432, 156256, 1, 822736, 986448, 156256, 1, 822752, 986464, 156256, 1, 822768, 986480, 156256, 1,
  985856, 1149568, 156160, 3, 985872, 1149584, 156160, 3, 985888, 1149600, 156160, 3, 985904, 1149616, 156160, 3,
  985984, 1149696, 156160, 3, 986000, 1149712, 156160, 3, 986016, 1149728, 156160, 3, 986032, 1149744, 156160, 3,
  986112, 1149824, 156160, 3, 986128, 1149840, 156160, 3, 986144, 1149856, 156160, 3, 986160, 1149872, 156160, 3,
  1149440, 1149952, 156160, 3, 1149456, 1149968, 156160, 3, 1149472, 1149984, 156160, 3, 1149488, 1150000, 156160, 3,
  985920, 1149632, 156192, 3, 985936, 1149648, 156192, 3, 985952, 1149664, 156192, 3, 985968, 1149680, 156192, 3,
  986048, 1149760, 156192, 3, 986064, 1149776, 156192, 3, 986080, 1149792, 156192, 3, 986096, 1149808, 156192, 3,
  986176, 1149888, 156192, 3, 986192, 1149904, 156192, 3, 986208, 1149920, 156192, 3, 986224, 1149936, 156192, 3,
  1149504, 1150016, 156192, 3, 1149520, 1150032, 156192, 3, 1149536, 1150048, 156192, 3, 1149552, 1150064, 156192, 3,
  986496, 1150208, 156224, 0, 986512, 1150224, 156224, 0, 986528, 1150240, 156224, 0, 986544, 1150256, 156224, 0,
  986624, 1150336, 156224, 0, 986640, 1150352, 156224, 0, 986656, 1150368, 156224, 0, 986672, 1150384, 156224, 0,
  986752, 1150464, 156224, 0, 986768, 1150480, 156224, 0, 986784, 1150496, 156224, 0, 986800, 1150512, 156224, 0,
  1150080, 1150592, 156224, 0, 1150096, 1150608, 156224, 0, 1150112, 1150624, 156224, 0, 1150128, 1150640, 156224, 0,
  986560, 1150272, 156256, 0, 986576, 1150288, 156256, 0, 986592, 1150304, 156256, 0, 986608, 1150320, 156256, 0,
  986688, 1150400, 156256, 0, 986704, 1150416, 156256, 0, 986720, 1150432, 156256, 0, 986736, 1150448, 156256, 0,
  986816, 1150528, 156256, 0, 986832, 1150544, 156256, 0, 986848, 1150560, 156256, 0, 986864, 1150576, 156256, 0,
  1150144, 1150656, 156256, 0, 1150160, 1150672, 156256, 0, 1150176, 1150688, 156256, 0, 1150192, 1150704, 156256, 0,
  0, 128, 156160, 0, 0, 128, 156164, 0, 0, 128, 156168, 0, 0, 128, 156172, 0,
  0, 128, 156176, 0, 0, 128, 156180, 0, 0, 128, 156184, 0, 0, 128, 156188, 0,
  0, 128, 156192, 0, 0, 128, 156196, 0, 0, 128, 156200, 0, 0, 128, 156204, 0,
  0, 128, 156208, 0, 0, 128, 156212, 0, 0, 128, 156216, 0, 0, 128, 156220, 0,
  32, 160, 156160, 0, 32, 160, 156164, 0, 32, 160, 156168, 0, 32, 160, 156172, 0,
  32, 160, 156176, 0, 32, 160, 156180, 0, 32, 160, 156184, 0, 32, 160, 156188, 0,
  32, 160, 156192, 0, 32, 160, 156196, 0, 32, 160, 156200, 0, 32, 160, 156204, 0,
  32, 160, 156208, 0, 32, 160, 156212, 0, 32, 160, 156216, 0, 32, 160, 156220, 0,
  64, 192, 156160, 0, 64, 192, 156164, 0, 64, 192, 156168, 0, 64, 192, 156172, 0,
  64, 192, 156176, 0, 64, 192, 156180, 0, 64, 192, 156184, 0, 64, 192, 156188, 0,
  64, 192, 156192, 0, 64, 192, 156196, 0, 64, 192, 156200, 0, 64, 192, 156204, 0,
  64, 192, 156208, 0, 64, 192, 156212, 0, 64, 192, 156216, 0, 64, 192, 156220, 0,
  96, 224, 156160, 0, 96, 224, 156164, 0, 96, 224, 156168, 0, 96, 224, 156172, 0,
  96, 224, 156176, 0, 96, 224, 156180, 0, 96, 224, 156184, 0, 96, 224, 156188, 0,
  96, 224, 156192, 0, 96, 224, 156196, 0, 96, 224, 156200, 0, 96, 224, 156204, 0,
  96, 224, 156208, 0, 96, 224, 156212, 0, 96, 224, 156216, 0, 96, 224, 156220, 0,
  0, 14400, 28832, 40960, 16, 14416, 28848, 40960, 32, 14432, 28800, 41088, 48, 14448, 28816, 41088,
  64, 14464, 31776, 41216, 80, 14480, 31792, 41216, 96, 14496, 31744, 41344, 112, 14512, 31760, 41344,
  128, 17408, 31840, 41472, 144, 17424, 31856, 41472, 160, 17440, 31808, 41600, 176, 17456, 31824, 41600,
  3072, 17472, 31904, 41728, 3088, 17488, 31920, 41728, 3104, 17504, 31872, 41856, 3120, 17520, 31888, 41856,
  3136, 17536, 34848, 81920, 3152, 17552, 34864, 81920, 3168, 17568, 34816, 82048, 3184, 17584, 34832, 82048,
  3200, 20480, 34912, 82176, 3216, 20496, 34928, 82176, 3232, 20512, 34880, 82304, 3248, 20528, 34896, 82304,
  6144, 20544, 34976, 82432, 6160, 20560, 34992, 82432, 6176, 20576, 34944, 82560, 6192, 20592, 34960, 82560,
  6208, 20608, 37920, 82688, 6224, 20624, 37936, 82688, 6240, 20640, 37888, 82816, 6256, 20656, 37904, 82816,
  6272, 23584, 37984, 0, 6288, 23600, 38000, 0, 6304, 23552, 37952, 40960, 6320, 23568, 37968, 40960,
  9216, 23648, 38048, 40960, 9232, 23664, 38064, 40960, 9248, 23616, 38016, 40960, 9264, 23632, 38032, 40960,
  9280, 23712, 40992, 40960, 9296, 23728, 41008, 40960, 9312, 23680, 40960, 40960, 9328, 23696, 40976, 40960,
  9344, 26656, 41056, 40960, 9360, 26672, 41072, 40960, 9376, 26624, 41024, 40960, 9392, 26640, 41040, 40960,
  12288, 26720, 41120, 40960, 12304, 26736, 41136, 40960, 12320, 26688, 41088, 40960, 12336, 26704, 41104, 40960,
  12352, 26784, 44064, 40960, 12368, 26800, 44080, 40960, 12384, 26752, 44032, 40960, 12400, 26768, 44048, 40960,
  12416, 29728, 44128, 40960, 12432, 29744, 44144, 40960, 12448, 29696, 44096, 40960, 12464, 29712, 44112, 40960,
  15360, 29792, 44192, 40960, 15376, 29808, 44208, 40960, 15392, 29760, 44160, 40960, 15408, 29776, 44176, 40960,
  0, 2048, 0, 0, 16, 2056, 1280, 16, 32, 2064, 2560, 32, 48, 2072, 3840, 48,
  64, 2080, 5120, 64, 80, 2088, 6400, 80, 96, 2096, 7680, 96, 112, 2104, 8960, 112,
  128, 2112, 10240, 128, 144, 2120, 11520, 144, 160, 2128, 12800, 160, 176, 2136, 14080, 176,
  192, 2144, 15360, 192, 208, 2152, 16640, 208, 224, 2160, 17920, 224, 240, 2168, 19200, 240,
  512, 4608, 40, 256, 528, 4616, 1320, 272, 544, 4624, 2600, 288, 560, 4632, 3880, 304,
  576, 4640, 5160, 320, 592, 4648, 6440, 336, 608, 4656, 7720, 352, 624, 4664, 9000, 368,
  640, 4672, 10280, 384, 656, 4680, 11560, 400, 672, 4688, 12840, 416, 688, 4696, 14120, 432,
  704, 4704, 15400, 448, 720, 4712, 16680, 464, 736, 4720, 17960, 480, 752, 4728, 19240, 496,
  2560, 7168, 80, 512, 2576, 7176, 1360, 528, 2592, 7184, 2640, 544, 2608, 7192, 3920, 560,
  2624, 7200, 5200, 576, 2640, 7208, 6480, 592, 2656, 7216, 7760, 608, 2672, 7224, 9040, 624,
  2688, 7232, 10320, 640, 2704, 7240, 11600, 656, 2720, 7248, 12880, 672, 2736, 7256, 14160, 688,
  2752, 7264, 15440, 704, 2768, 7272, 16720, 720, 2784, 7280, 18000, 736, 2800, 7288, 19280, 752,
  3072, 9728, 120, 768, 3088, 9736, 1400, 784, 3104, 9744, 2680, 800, 3120, 9752, 3960, 816,
  3136, 9760, 5240, 832, 3152, 9768, 6520, 848, 3168, 9776, 7800, 864, 3184, 9784, 9080, 880,
  3200, 9792, 10360, 896, 3216, 9800, 11640, 912, 3232, 9808, 12920, 928, 3248, 9816, 14200, 944,
  3264, 9824, 15480, 960, 3280, 9832, 16760, 976, 3296, 9840, 18040, 992, 3312, 9848, 19320, 1008,
  0, 0, 0, 0, 2048, 128, 0, 16, 128, 256, 0, 32, 2176, 384, 0, 48,
  256, 512, 0, 64, 2304, 640, 0, 80, 384, 768, 0, 96, 2432, 896, 0, 112,
  0, 1024, 0, 128, 128, 1152, 0, 144, 256, 1280, 0, 160, 384, 1408, 0, 176,
  512, 1536, 0, 192, 640, 1664, 0, 208, 768, 1792, 0, 224, 896, 1920, 0, 240,
  1024, 2048, 40, 0, 1152, 2176, 40, 16, 1280, 2304, 40, 32, 1408, 2432, 40, 48,
  6144, 2560, 40, 64, 6272, 2688, 40, 80, 6400, 2816, 40, 96, 6528, 2944, 40, 112,
  6656, 3072, 40, 128, 6784, 3200, 40, 144, 6912, 3328, 40, 160, 7040, 3456, 40, 176,
  7168, 3584, 40, 192, 7296, 3712, 40, 208, 7424, 3840, 40, 224, 7552, 3968, 40, 240,
  7680, 0, 80, 0, 7808, 128, 80, 16, 7936, 256, 80, 32, 8064, 384, 80, 48,
  8192, 512, 80, 64, 8320, 640, 80, 80, 8448, 768, 80, 96, 8576, 896, 80, 112,
  8576, 1024, 80, 128, 8576, 1152, 80, 144, 8576, 1280, 80, 160, 8576, 1408, 80, 176,
  8576, 1536, 80, 192, 8576, 1664, 80, 208, 8576, 1792, 80, 224, 8576, 1920, 80, 240,
  8576, 2048, 120, 0, 8576, 2176, 120, 16, 8576, 2304, 120, 32, 8576, 2432, 120, 48,
  8576, 2560, 120, 64, 8576, 2688, 120, 80, 8576, 2816, 120, 96, 8576, 2944, 120, 112,
  8576, 3072, 120, 128, 8576, 3200, 120, 144, 8576, 3328, 120, 160, 8576, 3456, 120, 176,
  8576, 3584, 120, 192, 8576, 3712, 120, 208, 8576, 3840, 120, 224, 8576, 3968, 120, 240,
  0, 7744, 15488, 24352, 16, 7760, 15504, 24368, 32, 7776, 16640, 24384, 48, 7792, 16656, 24400,
  64, 7808, 16672, 24416, 80, 7824, 16688, 24432, 96, 8960, 16704, 24448, 112, 8976, 16720, 24464,
  128, 8992, 16736, 25600, 144, 9008, 16752, 25616, 1280, 9024, 16768, 25632, 1296, 9040, 16784, 25648,
  1312, 9056, 17920, 25664, 1328, 9072, 17936, 25680, 1344, 9088, 17952, 25696, 1360, 9104, 17968, 25712,
  1376, 10240, 17984, 25728, 1392, 10256, 18000, 25744, 1408, 10272, 18016, 26880, 1424, 10288, 18032, 26896,
  2560, 10304, 18048, 26912, 2576, 10320, 18064, 26928, 2592, 10336, 19200, 26944, 2608, 10352, 19216, 26960,
  2624, 10368, 19232, 26976, 2640, 10384, 19248, 26992, 2656, 11520, 19264, 27008, 2672, 11536, 19280, 27024,
  2688, 11552, 19296, 28160, 2704, 11568, 19312, 28176, 3840, 11584, 19328, 28192, 3856, 11600, 19344, 28208,
  3872, 11616, 20480, 28224, 3888, 11632, 20496, 28240, 3904, 11648, 20512, 28256, 3920, 11664, 20528, 28272,
  3936, 12800, 20544, 28288, 3952, 12816, 20560, 28304, 3968, 12832, 20576, 29440, 3984, 12848, 20592, 29456,
  5120, 12864, 20608, 29472, 5136, 12880, 20624, 29488, 5152, 12896, 21760, 29504, 5168, 12912, 21776, 29520,
  5184, 12928, 21792, 29536, 5200, 12944, 21808, 29552, 5216, 14080, 21824, 29568, 5232, 14096, 21840, 29584,
  5248, 14112, 21856, 30720, 5264, 14128, 21872, 30736, 6400, 14144, 21888, 30752, 6416, 14160, 21904, 30768,
  6432, 14176, 23040, 30784, 6448, 14192, 23056, 30800, 6464, 14208, 23072, 30816, 6480, 14224, 23088, 30832,
  6496, 15360, 23104, 30848, 6512, 15376, 23120, 30864, 6528, 15392, 23136, 32000, 6544, 15408, 23152, 32016,
  7680, 15424, 23168, 32032, 7696, 15440, 23184, 32048, 7712, 15456, 24320, 32064, 7728, 15472, 24336, 32080,
  32096, 0, 0, 0, 32112, 40, 0, 0, 32128, 80, 0, 0, 32144, 120, 0, 0,
  33280, 160, 0, 0, 33296, 200, 0, 0, 33312, 240, 0, 0, 33328, 280, 0, 0,
  33344, 320, 0, 0, 33360, 360, 0, 0, 33376, 400, 0, 0, 33392, 440, 0, 0,
  33408, 480, 0, 0, 33424, 520, 0, 0, 34560, 560, 0, 0, 34576, 600, 0, 0,
  34592, 640, 0, 0, 34608, 680, 0, 0, 34624, 720, 0, 0, 34640, 760, 0, 0,
  34656, 800, 0, 0, 34672, 840, 0, 0, 34688, 880, 0, 0, 34704, 920, 0, 0,
  35840, 960, 0, 0, 35856, 1000, 0, 0, 35872, 1040, 0, 0, 35888, 1080, 0, 0,
  35904, 1120, 0, 0, 35920, 1160, 0, 0, 35936, 1200, 0, 0, 35952, 1240, 0, 0,
  35968, 1280, 0, 0, 35984, 1320, 0, 0, 37120, 1360, 0, 0, 37136, 1400, 0, 0,
  37152, 1440, 0, 0, 37168, 1480, 0, 0, 37184, 1520, 0, 0, 37200, 1560, 0, 0,
  37216, 1600, 0, 0, 37232, 1640, 0, 0, 37248, 1680, 0, 0, 37264, 1720, 0, 0,
  38400, 1760, 0, 0, 38416, 1800, 0, 0, 38432, 1840, 0, 0, 38448, 1880, 0, 0,
  38464, 1920, 0, 0, 38480, 1960, 0, 0, 38496, 2000, 0, 0, 38512, 2040, 0, 0,
  38528, 2080, 0, 0, 38544, 2120, 0, 0, 39680, 2160, 0, 0, 39696, 2200, 0, 0,
  39712, 2240, 0, 0, 39728, 2280, 0, 0, 39744, 2320, 0, 0, 39760, 2360, 0, 0,
  39776, 2400, 0, 0, 39792, 2440, 0, 0, 39808, 2480, 0, 0, 39824, 2520, 0, 0,
  0, 7744, 30848, 14112, 16, 7760, 30864, 14128, 32, 7776, 32000, 14144, 48, 7792, 32016, 14160,
  64, 7808, 32032, 14176, 80, 7824, 32048, 14192, 96, 8960, 32064, 14208, 112, 8976, 32080, 14224,
  128, 8992, 32096, 15360, 144, 9008, 32112, 15376, 1280, 9024, 32128, 15392, 1296, 9040, 32144, 15408,
  1312, 9056, 33280, 15424, 1328, 9072, 33296, 15440, 1344, 9088, 33312, 15456, 1360, 9104, 33328, 15472,
  1376, 25600, 33344, 15488, 1392, 25616, 33360, 15504, 1408, 25632, 33376, 16640, 1424, 25648, 33392, 16656,
  2560, 25664, 33408, 16672, 2576, 25680, 33424, 16688, 2592, 25696, 34560, 16704, 2608, 25712, 34576, 16720,
  2624, 25728, 34592, 16736, 2640, 25744, 34608, 16752, 2656, 26880, 34624, 16768, 2672, 26896, 34640, 16784,
  2688, 26912, 34656, 17920, 2704, 26928, 34672, 17936, 3840, 26944, 34688, 17952, 3856, 26960, 34704, 17968,
  3872, 26976, 10240, 17984, 3888, 26992, 10256, 18000, 3904, 27008, 10272, 18016, 3920, 27024, 10288, 18032,
  3936, 28160, 10304, 18048, 3952, 28176, 10320, 18064, 3968, 28192, 10336, 19200, 3984, 28208, 10352, 19216,
  5120, 28224, 10368, 19232, 5136, 28240, 10384, 19248, 5152, 28256, 11520, 19264, 5168, 28272, 11536, 19280,
  5184, 28288, 11552, 19296, 5200, 28304, 11568, 19312, 5216, 29440, 11584, 19328, 5232, 29456, 11600, 19344,
  5248, 29472, 11616, 35840, 5264, 29488, 11632, 35856, 6400, 29504, 11648, 35872, 6416, 29520, 11664, 35888,
  6432, 29536, 12800, 35904, 6448, 29552, 12816, 35920, 6464, 29568, 12832, 35936, 6480, 29584, 12848, 35952,
  6496, 30720, 12864, 35968, 6512, 30736, 12880, 35984, 6528, 30752, 12896, 37120, 6544, 30768, 12912, 37136,
  7680, 30784, 12928, 37152, 7696, 30800, 12944, 37168, 7712, 30816, 14080, 37184, 7728, 30832, 14096, 37200,
  37216, 0, 0, 0, 37232, 40, 0, 0, 37248, 80, 0, 0, 37264, 120, 0, 0,
  38400, 160, 0, 0, 38416, 200, 0, 0, 38432, 240, 0, 0, 38448, 280, 0, 0,
  38464, 320, 0, 0, 38480, 360, 0, 0, 38496, 400, 0, 0, 38512, 440, 0, 0,
  38528, 480, 0, 0, 38544, 520, 0, 0, 39680, 560, 0, 0, 39696, 600, 0, 0,
  39712, 640, 0, 0, 39728, 680, 0, 0, 39744, 720, 0, 0, 39760, 760, 0, 0,
  39776, 800, 0, 0, 39792, 840, 0, 0, 39808, 880, 0, 0, 39824, 920, 0, 0,
  40960, 960, 0, 0, 40976, 1000, 0, 0, 40992, 1040, 0, 0, 41008, 1080, 0, 0,
  41024, 1120, 0, 0, 41040, 1160, 0, 0, 41056, 1200, 0, 0, 41072, 1240, 0, 0,
  41088, 1280, 0, 0, 41104, 1320, 0, 0, 42240, 1360, 0, 0, 42256, 1400, 0, 0,
  42272, 1440, 0, 0, 42288, 1480, 0, 0, 42304, 1520, 0, 0, 42320, 1560, 0, 0,
  42336, 1600, 0, 0, 42352, 1640, 0, 0, 42368, 1680, 0, 0, 42384, 1720, 0, 0,
  43520, 1760, 0, 0, 43536, 1800, 0, 0, 43552, 1840, 0, 0, 43568, 1880, 0, 0,
  43584, 1920, 0, 0, 43600, 1960, 0, 0, 43616, 2000, 0, 0, 43632, 2040, 0, 0,
  43648, 2080, 0, 0, 43664, 2120, 0, 0, 44800, 2160, 0, 0, 44816, 2200, 0, 0,
  44832, 2240, 0, 0, 44848, 2280, 0, 0, 44864, 2320, 0, 0, 44880, 2360, 0, 0,
  44896, 2400, 0, 0, 44912, 2440, 0, 0, 44928, 2480, 0, 0, 44944, 2520, 0, 0,
  20480, 48704, 97408, 126752, 20496, 48720, 97424, 126768, 20512, 48736, 98560, 126784, 20528, 48752, 98576, 126800,
  20544, 48768, 98592, 126816, 20560, 48784, 98608, 126832, 20576, 49920, 98624, 126848, 20592, 49936, 98640, 126864,
  20608, 49952, 98656, 148480, 20624, 49968, 98672, 148496, 21760, 49984, 98688, 148512, 21776, 50000, 98704, 148528,
  21792, 50016, 99840, 148544, 21808, 50032, 99856, 148560, 21824, 50048, 99872, 148576, 21840, 50064, 99888, 148592,
  21856, 71680, 99904, 148608, 21872, 71696, 99920, 148624, 21888, 71712, 99936, 149760, 21904, 71728, 99952, 149776,
  23040, 71744, 99968, 149792, 23056, 71760, 99984, 149808, 23072, 71776, 101120, 149824, 23088, 71792, 101136, 149840,
  23104, 71808, 101152, 149856, 23120, 71824, 101168, 149872, 23136, 72960, 101184, 149888, 23152, 72976, 101200, 149904,
  23168, 72992, 101216, 151040, 23184, 73008, 101232, 151056, 24320, 73024, 101248, 151072, 24336, 73040, 101264, 151088,
  24352, 73056, 122880, 151104, 24368, 73072, 122896, 151120, 24384, 73088, 122912, 151136, 24400, 73104, 122928, 151152,
  24416, 74240, 122944, 151168, 24432, 74256, 122960, 151184, 24448, 74272, 122976, 152320, 24464, 74288, 122992, 152336,
  46080, 74304, 123008, 152352, 46096, 74320, 123024, 152368, 46112, 74336, 124160, 152384, 46128, 74352, 124176, 152400,
  46144, 74368, 124192, 152416, 46160, 74384, 124208, 152432, 46176, 75520, 124224, 152448, 46192, 75536, 124240, 152464,
  46208, 75552, 124256, 174080, 46224, 75568, 124272, 174096, 47360, 75584, 124288, 174112, 47376, 75600, 124304, 174128,
  47392, 75616, 125440, 174144, 47408, 75632, 125456, 174160, 47424, 75648, 125472, 174176, 47440, 75664, 125488, 174192,
  47456, 97280, 125504, 174208, 47472, 97296, 125520, 174224, 47488, 97312, 125536, 175360, 47504, 97328, 125552, 175376,
  48640, 97344, 125568, 175392, 48656, 97360, 125584, 175408, 48672, 97376, 126720, 175424, 48688, 97392, 126736, 175440,
  175456, 0, 0, 0, 175472, 40, 0, 0, 175488, 80, 0, 0, 175504, 120, 0, 0,
  176640, 160, 0, 0, 176656, 200, 0, 0, 176672, 240, 0, 0, 176688, 280, 0, 0,
  176704, 320, 0, 0, 176720, 360, 0, 0, 176736, 400, 0, 0, 176752, 440, 0, 0,
  176768, 480, 0, 0, 176784, 520, 0, 0, 177920, 560, 0, 0, 177936, 600, 0, 0,
  177952, 640, 0, 0, 177968, 680, 0, 0, 177984, 720, 0, 0, 178000, 760, 0, 0,
  178016, 800, 0, 0, 178032, 840, 0, 0, 178048, 880, 0, 0, 178064, 920, 0, 0,
  199680, 960, 0, 0, 199696, 1000, 0, 0, 199712, 1040, 0, 0, 199728, 1080, 0, 0,
  199744, 1120, 0, 0, 199760, 1160, 0, 0, 199776, 1200, 0, 0, 199792, 1240, 0, 0,
  199808, 1280, 0, 0, 199824, 1320, 0, 0, 200960, 1360, 0, 0, 200976, 1400, 0, 0,
  200992, 1440, 0, 0, 201008, 1480, 0, 0, 201024, 1520, 0, 0, 201040, 1560, 0, 0,
  201056, 1600, 0, 0, 201072, 1640, 0, 0, 201088, 1680, 0, 0, 201104, 1720, 0, 0,
  202240, 1760, 0, 0, 202256, 1800, 0, 0, 202272, 1840, 0, 0, 202288, 1880, 0, 0,
  202304, 1920, 0, 0, 202320, 1960, 0, 0, 202336, 2000, 0, 0, 202352, 2040, 0, 0,
  202368, 2080, 0, 0, 202384, 2120, 0, 0, 203520, 2160, 0, 0, 203536, 2200, 0, 0,
  203552, 2240, 0, 0, 203568, 2280, 0, 0, 203584, 2320, 0, 0, 203600, 2360, 0, 0,
  203616, 2400, 0, 0, 203632, 2440, 0, 0, 203648, 2480, 0, 0, 203664, 2520, 0, 0};

#define MAIN_ASM \
    \
  "s_load_dwordx16 s[8:23], s[4:5], 0x0\n" \
  "s_load_dwordx2 s[24:25], s[4:5], 0x40\n" \
  "v_readfirstlane_b32 s28, v0\n" \
  "v_and_b32 v0, 63, v0\n" \
  "s_lshr_b32 s34, s26, 3\n" \
  "s_lshr_b32 s28, s28, 6\n" \
  "s_and_b32 s29, s28, 3\n" \
  "s_getpc_b64 s[36:37]\n" \
  ".Lpc_ref:\n" \
  "s_lshl3_add_u32 s35, s34, s28\n" \
  "s_lshl_b32 s35, s35, 7\n" \
  "s_min_u32 s35, s35, .Lcode_last-.Lpc_ref\n" \
  "v_mov_b32 v1, s35\n" \
  "s_mov_b64 exec, 1\n" \
  "global_load_dword v250, v1, s[36:37]\n" \
  "s_mov_b64 exec, -1\n" \
  "s_and_b32 s30, s26, 1\n" \
  "s_and_b32 s35, s34, 7\n" \
  "s_lshl3_add_u32 s30, s30, s35\n" \
  "s_bfe_u32 s31, s26, 0x20001\n" \
  "s_lshr_b32 s35, s34, 3\n" \
  "s_lshl2_add_u32 s31, s31, s35\n" \
  "s_mul_i32 s33, s31, 13\n" \
  "s_lshr_b32 s33, s33, 6\n" \
  "s_cmp_ge_u32 s28, 4\n" \
  "s_cbranch_scc1 1f\n" \
    \
  "v_lshrrev_b32 v1, 5, v0\n" \
  "v_mul_u32_u24 v1, 0x400, v1\n" \
  "v_lshl_add_u32 v1, v0, 3, v1\n" \
  "s_lshl4_add_u32 s35, s30, s31\n" \
  "s_lshl2_add_u32 s35, s35, s29\n" \
  "s_mul_i32 s35, s35, 2560\n" \
  "v_add_u32 v1, s35, v1\n" \
  "s_mul_i32 s35, s31, 192\n" \
  "v_min_u32 v2, 47, v0\n" \
  "v_lshl_add_u32 v2, v2, 2, s35\n" \
  "s_cmp_eq_u32 s33, 2\n" \
  "s_cselect_b32 s35, 1024, 0\n" \
  "v_lshl_add_u32 v3, v0, 4, s35\n" \
  "s_lshl2_add_u32 s35, s31, s29\n" \
  "s_mul_i32 s36, s35, 52\n" \
  "s_lshr_b32 s36, s36, 8\n" \
  "s_sub_u32 s36, s33, s36\n" \
  "s_mul_i32 s36, s36, 5\n" \
  "s_add_u32 s35, s35, s36\n" \
  "s_min_u32 s35, s35, 15\n" \
  "s_add_u32 s35, s35, 2\n" \
  "s_lshl_b32 s35, s35, 10\n" \
  "v_lshl_add_u32 v4, v0, 4, s35\n" \
  "s_movk_i32 s35, 18432\n" \
  "v_lshl_add_u32 v5, v0, 4, s35\n" \
  "global_load_dwordx4 v[156:159], v3, s[6:7]\n" \
  "global_load_dwordx4 v[164:167], v4, s[6:7]\n" \
  "global_load_dwordx4 v[240:243], v5, s[6:7]\n" \
  "s_and_b32 s35, s26, 1\n" \
  "s_lshl1_add_u32 s35, s34, s35\n" \
  "s_lshr_b32 s36, s29, 1\n" \
  "s_lshl1_add_u32 s35, s35, s36\n" \
  "s_lshl2_add_u32 s35, s35, 128\n" \
  "v_bfe_u32 v6, v0, 3, 2\n" \
  "v_add_u32 v6, s35, v6\n" \
  "s_lshr_b32 s35, s31, 2\n" \
  "s_cmp_eq_u32 s35, 3\n" \
  "s_cselect_b32 s37, 5, 7\n" \
  "s_bitcmp0_b32 s29, 0\n" \
  "s_cselect_b32 s37, s37, 5\n" \
  "s_cselect_b32 s36, 8, 6\n" \
  "s_mul_i32 s35, s35, s36\n" \
  "s_cselect_b32 s36, 30, 24\n" \
  "v_and_b32 v7, 7, v0\n" \
  "v_min_u32 v7, s37, v7\n" \
  "v_mad_u32_u24 v6, v6, s36, v7\n" \
  "v_add_lshl_u32 v6, v6, s35, 7\n" \
  "v_mov_b32 v7, 0\n" \
  "s_waitcnt lgkmcnt(0)\n" \
  "s_cselect_b64 s[36:37], s[10:11], s[14:15]\n" \
  "s_sleep 8\n" \
  "global_load_dwordx2 v[200:201], v1, s[8:9] offset:0\n" \
  "global_load_dwordx2 v[202:203], v1, s[8:9] offset:256\n" \
  "global_load_dwordx2 v[204:205], v1, s[8:9] offset:512\n" \
  "global_load_dwordx2 v[206:207], v1, s[8:9] offset:768\n" \
  "global_load_dwordx2 v[208:209], v1, s[8:9] offset:1024\n" \
  "global_load_dwordx2 v[210:211], v7, s[24:25] sc1\n" \
  "global_load_dword v212, v2, s[16:17]\n" \
  "global_load_dword v251, v6, s[36:37]\n" \
  "s_branch .Lqkv\n" \
    \
  "1:\n" \
  "s_setprio 3\n" \
  "v_lshrrev_b32 v1, 3, v0\n" \
  "v_bfe_u32 v2, v0, 4, 1\n" \
  "v_and_or_b32 v2, v1, 4, v2\n" \
  "v_and_b32 v3, 7, v0\n" \
  "v_xor_b32 v2, v2, v3\n" \
  "v_mul_u32_u24 v1, 0x500, v1\n" \
  "v_lshl_add_u32 v216, v2, 4, v1\n" \
  "v_xor_b32 v1, 32, v216\n" \
  "v_add_u32 v217, 9216, v1\n" \
  "v_xor_b32 v1, 64, v216\n" \
  "v_add_u32 v218, 18432, v1\n" \
  "v_xor_b32 v1, 96, v216\n" \
  "v_add_u32 v219, 27648, v1\n" \
  "s_lshl2_add_u32 s35, s30, s29\n" \
  "s_mul_i32 s35, s35, 40960\n" \
  "s_mov_b32 s56, 0\n" \
  "s_lshl_b32 s59, s29, 12\n" \
  "s_mov_b32 s42, 40960\n" \
  "s_mov_b32 s43, 0x20000\n" \
  "s_mov_b32 s47, 0x20000\n" \
  "s_cmp_lt_u32 s30, 12\n" \
  "s_cselect_b32 s46, 1966080, 0\n" \
  "s_cmp_eq_u32 s33, 3\n" \
  "s_cselect_b32 s46, s46, 2457600\n" \
  "s_cselect_b32 s36, 12, 15\n" \
  "s_cselect_b32 s37, s30, s31\n" \
  "s_waitcnt lgkmcnt(0)\n" \
  "s_cselect_b64 s[44:45], s[14:15], s[10:11]\n" \
  "s_add_u32 s40, s8, s35\n" \
  "s_addc_u32 s41, s9, 0\n" \
  "s_and_b32 s41, s41, 0xffff\n" \
  "s_add_u32 m0, s59, 0\n" \
  "s_nop 0\n" \
  "buffer_load_dwordx4 v216, s[40:43], s56 offen offset:0 lds\n" \
  "s_and_b32 s45, s45, 0xffff\n" \
  "s_lshl_b32 s36, s36, 8\n" \
  "s_lshl_b32 s61, s36, 6\n" \
  "s_mul_i32 s57, s29, s36\n" \
  "s_lshl4_add_u32 s57, s37, s57\n" \
  "s_lshl_b32 s57, s57, 4\n" \
  "v_lshrrev_b32 v1, 4, v0\n" \
  "buffer_load_dwordx4 v217, s[40:43], s56 offen offset:1024 lds\n" \
  "v_and_b32 v2, 15, v0\n" \
  "v_lshlrev_b32 v3, 1, v1\n" \
  "v_xor_b32 v2, v2, v3\n" \
  "v_mul_u32_u24 v1, s36, v1\n" \
  "v_lshl_add_u32 v220, v2, 4, v1\n" \
  "s_lshl_b32 s36, s36, 2\n" \
  "s_sub_u32 s36, s36, 1024\n" \
  "buffer_load_dwordx4 v218, s[40:43], s56 offen offset:2048 lds\n" \
  "v_add_u32 v221, s36, v220\n" \
  "v_xor_b32 v1, 0x80, v221\n" \
  "v_add_u32 v222, s36, v1\n" \
  "v_add_u32 v223, s36, v222\n" \
  "buffer_load_dwordx4 v219, s[40:43], s56 offen offset:3072 lds\n" \
  "s_add_u32 s56, s56, 128\n" \
  "s_add_u32 m0, s59, 65536\n" \
  "s_nop 0\n" \
  "buffer_load_dwordx4 v220, s[44:47], s57 offen offset:0 lds\n" \
  "buffer_load_dwordx4 v221, s[44:47], s57 offen offset:1024 lds\n" \
  "buffer_load_dwordx4 v222, s[44:47], s57 offen offset:2048 lds\n" \
  "buffer_load_dwordx4 v223, s[44:47], s57 offen offset:3072 lds\n" \
  "s_add_u32 s57, s57, s61\n" \
  "s_add_u32 m0, s59, 16384\n" \
  "s_nop 0\n" \
  "buffer_load_dwordx4 v216, s[40:43], s56 offen offset:0 lds\n" \
  "buffer_load_dwordx4 v217, s[40:43], s56 offen offset:1024 lds\n" \
  "buffer_load_dwordx4 v218, s[40:43], s56 offen offset:2048 lds\n" \
  "buffer_load_dwordx4 v219, s[40:43], s56 offen offset:3072 lds\n" \
  "s_add_u32 s56, s56, 128\n" \
  "s_add_u32 m0, s59, 81920\n" \
  "s_nop 0\n" \
  "buffer_load_dwordx4 v220, s[44:47], s57 offen offset:0 lds\n" \
  "buffer_load_dwordx4 v221, s[44:47], s57 offen offset:1024 lds\n" \
  "buffer_load_dwordx4 v222, s[44:47], s57 offen offset:2048 lds\n" \
  "buffer_load_dwordx4 v223, s[44:47], s57 offen offset:3072 lds\n" \
  "s_add_u32 s57, s57, s61\n" \
    \
  "s_mul_i32 s58, s29, 49152\n" \
  "s_mul_i32 s34, s31, 192\n" \
  "s_add_u32 s58, s58, s34\n" \
  "s_mul_i32 s60, s29, 3072\n" \
  "s_mov_b32 s48, s14\n" \
  "s_and_b32 s49, s15, 0xffff\n" \
  "s_mov_b32 s50, 1966080\n" \
  "s_mov_b32 s51, 0x20000\n" \
  "s_mov_b32 s52, s24\n" \
  "s_and_b32 s53, s25, 0xffff\n" \
  "s_mov_b32 s54, 20152320\n" \
  "s_mov_b32 s55, 0x20000\n" \
  "s_cmp_eq_u32 s33, 0\n" \
  "s_cselect_b32 s65, 1051011932, 1.0\n" \
  "s_lshl_b32 s68, s30, 11\n" \
  "s_lshl_b32 s34, s29, 9\n" \
  "s_add_u32 s68, s68, s34\n" \
  "s_add_u32 s68, s68, 32768\n" \
  "s_mul_i32 s69, s31, 768\n" \
  "s_add_u32 s69, s69, 4096\n" \
  "s_mul_i32 s70, s30, 393216\n" \
  "s_mul_i32 s34, s29, 98304\n" \
  "s_add_u32 s70, s70, s34\n" \
  "s_mul_i32 s34, s31, 3072\n" \
  "s_add_u32 s70, s70, s34\n" \
  "s_add_u32 s70, s70, 8454144\n" \
  "s_lshl_b32 s71, s30, 13\n" \
  "s_lshl_b32 s34, s29, 11\n" \
  "s_add_u32 s71, s71, s34\n" \
  "s_lshl_b32 s34, s31, 7\n" \
  "s_add_u32 s71, s71, s34\n" \
  "s_add_u32 s71, s71, 196608\n" \
    \
  "s_lshl1_add_u32 s74, s26, s29\n" \
  "s_add_u32 s34, s26, 512\n" \
  "s_cmp_eq_u32 s29, 2\n" \
  "s_cselect_b32 s74, s34, s74\n" \
  "s_cmp_eq_u32 s29, 3\n" \
  "s_cselect_b32 s74, 1023, s74\n" \
  "s_cmp_ge_u32 s74, 384\n" \
  "s_cselect_b32 s34, 23, 21\n" \
  "s_cmp_ge_u32 s74, 512\n" \
  "s_cselect_b32 s34, 25, s34\n" \
  "s_lshl_b32 s34, s34, 10\n" \
  "s_add_u32 s72, s6, s34\n" \
  "s_addc_u32 s73, s7, 0\n" \
  "s_lshr_b32 s39, s74, 3\n" \
  "s_and_b32 s27, s74, 7\n" \
  "s_mul_i32 s35, s27, 68\n" \
  "s_add_u32 s35, s35, s39\n" \
  "s_mul_i32 s36, s35, 5120\n" \
  "s_add_u32 s36, s36, 17367040\n" \
  "s_lshl_b32 s37, s35, 7\n" \
  "s_add_u32 s37, s37, 458752\n" \
  "s_cmp_ge_u32 s39, 48\n" \
  "s_cselect_b64 s[76:77], s[12:13], s[18:19]\n" \
  "s_cselect_b32 s78, 5, 12\n" \
  "s_cselect_b32 s34, 48, 0\n" \
  "s_cselect_b32 s35, 5, 4\n" \
  "s_cmp_ge_u32 s39, 64\n" \
  "s_cselect_b32 s34, 64, s34\n" \
  "s_cselect_b32 s35, 20, s35\n" \
  "s_sub_u32 s39, s39, s34\n" \
  "s_mul_i32 s39, s39, s35\n" \
  "s_mul_i32 s78, s78, 163840\n" \
  "s_and_b32 s77, s77, 0xffff\n" \
  "s_mov_b32 s79, 0x20000\n" \
  "s_mul_i32 s80, s39, 10240\n" \
  "s_mul_i32 s34, s27, 160\n" \
  "s_add_u32 s80, s80, s34\n" \
  "s_lshl_b32 s38, s29, 15\n" \
  "s_add_u32 s38, s38, 24576\n" \
  "s_cmp_ge_u32 s74, 544\n" \
  "s_cselect_b32 s78, 0, s78\n" \
  "s_cselect_b32 s36, 20152320, s36\n" \
  "s_cselect_b32 s37, 20152320, s37\n" \
  "s_add_u32 s34, s6, 19456\n" \
  "s_addc_u32 s35, s7, 0\n" \
    \
  "v_and_b32 v1, 15, v0\n" \
  "v_lshrrev_b32 v2, 4, v0\n" \
  "v_lshlrev_b32 v213, 4, v0\n" \
  "v_bfe_u32 v3, v1, 1, 1\n" \
  "v_lshrrev_b32 v4, 2, v1\n" \
  "v_and_or_b32 v3, v4, 2, v3\n" \
  "v_and_or_b32 v3, v1, 4, v3\n" \
  "v_xor_b32 v3, v3, v2\n" \
  "v_lshl_add_u32 v3, v1, 3, v3\n" \
  "v_lshl_add_u32 v224, v3, 4, s59\n" \
  "v_xor_b32 v225, 64, v224\n" \
  "v_and_b32 v226, 0x1f0, v213\n" \
  "s_lshl_b32 s64, s29, 7\n" \
  "s_add_u32 s64, s64, 155648\n" \
  "v_lshrrev_b32 v227, 2, v226\n" \
  "v_add_u32 v227, s64, v227\n" \
  "v_lshl_add_u32 v239, v1, 2, s64\n" \
  "v_lshl_add_u32 v4, v2, 3, v4\n" \
  "v_mul_u32_u24 v4, 192, v4\n" \
  "v_and_b32 v3, 3, v0\n" \
  "v_lshl_add_u32 v4, v3, 3, v4\n" \
  "v_and_b32 v3, 16, v0\n" \
  "v_lshlrev_b32 v3, 1, v3\n" \
  "v_add_u32 v240, v4, v3\n" \
  "v_sub_u32 v241, v4, v3\n" \
  "v_add_u32 v242, 65536, v240\n" \
  "v_add_u32 v243, 65536, v241\n" \
  "v_add_u32 v244, 131072, v240\n" \
  "v_add_u32 v245, 131072, v241\n" \
  "s_mul_i32 s64, s29, 384\n" \
  "s_add_u32 s64, s64, 157696\n" \
  "v_min_u32 v210, 752, v213\n" \
  "v_lshrrev_b32 v211, 1, v210\n" \
  "v_add_u32 v211, s64, v211\n" \
  "v_lshl_add_u32 v212, v2, 4, s64\n" \
  "v_mov_b32 v3, 157312\n" \
  "v_lshl_add_u32 v248, v2, 4, v3\n" \
  "v_mov_b32 v251, 157184\n" \
  "v_add_u32 v3, 0, v0\n" \
  "v_mul_u32_u24 v4, 171, v3\n" \
  "v_lshrrev_b32 v4, 11, v4\n" \
  "v_mad_i32_i24 v3, v4, -12, v3\n" \
  "v_lshrrev_b32 v236, 2, v4\n" \
  "v_and_b32 v236, 2, v236\n" \
  "v_xor_b32 v3, v3, v236\n" \
  "v_mul_u32_u24 v4, 192, v4\n" \
  "v_add_u32 v3, v3, v4\n" \
  "v_subrev_u32 v3, 0, v3\n" \
  "v_lshlrev_b32 v236, 4, v3\n" \
  "v_add_u32 v3, 64, v0\n" \
  "v_mul_u32_u24 v4, 171, v3\n" \
  "v_lshrrev_b32 v4, 11, v4\n" \
  "v_mad_i32_i24 v3, v4, -12, v3\n" \
  "v_lshrrev_b32 v237, 2, v4\n" \
  "v_and_b32 v237, 2, v237\n" \
  "v_xor_b32 v3, v3, v237\n" \
  "v_mul_u32_u24 v4, 192, v4\n" \
  "v_add_u32 v3, v3, v4\n" \
  "v_subrev_u32 v3, 64, v3\n" \
  "v_lshlrev_b32 v237, 4, v3\n" \
  "v_add_u32 v3, 128, v0\n" \
  "v_mul_u32_u24 v4, 171, v3\n" \
  "v_lshrrev_b32 v4, 11, v4\n" \
  "v_mad_i32_i24 v3, v4, -12, v3\n" \
  "v_lshrrev_b32 v238, 2, v4\n" \
  "v_and_b32 v238, 2, v238\n" \
  "v_xor_b32 v3, v3, v238\n" \
  "v_mul_u32_u24 v4, 192, v4\n" \
  "v_add_u32 v3, v3, v4\n" \
  "v_subrev_u32 v3, 128, v3\n" \
  "v_lshlrev_b32 v238, 4, v3\n" \
    \
  "s_waitcnt vmcnt(8)\n" \
  "s_waitcnt lgkmcnt(0)\n" \
  "s_barrier\n" \
  "ds_read_b128 v[0:3], v224 offset:0\n" \
  "ds_read_b128 v[4:7], v225 offset:2048\n" \
  "ds_read_b128 v[8:11], v225 offset:0\n" \
  "ds_read_b128 v[12:15], v224 offset:2048\n" \
  "s_add_u32 m0, s59, 32768\n" \
  "s_nop 0\n" \
  "buffer_load_dwordx4 v216, s[40:43], s56 offen offset:0 lds\n" \
  "buffer_load_dwordx4 v217, s[40:43], s56 offen offset:1024 lds\n" \
  "buffer_load_dwordx4 v218, s[40:43], s56 offen offset:2048 lds\n" \
  "buffer_load_dwordx4 v219, s[40:43], s56 offen offset:3072 lds\n" \
  "s_add_u32 s56, s56, 128\n" \
  "s_add_u32 m0, s59, 98304\n" \
  "s_nop 0\n" \
  "buffer_load_dwordx4 v220, s[44:47], s57 offen offset:0 lds\n" \
  "buffer_load_dwordx4 v221, s[44:47], s57 offen offset:1024 lds\n" \
  "buffer_load_dwordx4 v222, s[44:47], s57 offen offset:2048 lds\n" \
  "buffer_load_dwordx4 v223, s[44:47], s57 offen offset:3072 lds\n" \
  "s_add_u32 s57, s57, s61\n" \
    \
  "s_waitcnt vmcnt(8)\n" \
  "s_waitcnt lgkmcnt(0)\n" \
  "s_barrier\n" \
  "ds_read_b128 v[16:19], v224 offset:16384\n" \
  "ds_read_b128 v[20:23], v225 offset:18432\n" \
  "ds_read_b128 v[24:27], v225 offset:16384\n" \
  "ds_read_b128 v[28:31], v224 offset:18432\n" \
  "s_add_u32 m0, s59, 49152\n" \
  "s_nop 0\n" \
  "buffer_load_dwordx4 v216, s[40:43], s56 offen offset:0 lds\n" \
  "buffer_load_dwordx4 v217, s[40:43], s56 offen offset:1024 lds\n" \
  "buffer_load_dwordx4 v218, s[40:43], s56 offen offset:2048 lds\n" \
  "buffer_load_dwordx4 v219, s[40:43], s56 offen offset:3072 lds\n" \
  "s_add_u32 s56, s56, 128\n" \
  "s_add_u32 m0, s59, 114688\n" \
  "s_nop 0\n" \
  "buffer_load_dwordx4 v220, s[44:47], s57 offen offset:0 lds\n" \
  "buffer_load_dwordx4 v221, s[44:47], s57 offen offset:1024 lds\n" \
  "buffer_load_dwordx4 v222, s[44:47], s57 offen offset:2048 lds\n" \
  "buffer_load_dwordx4 v223, s[44:47], s57 offen offset:3072 lds\n" \
  "s_add_u32 s57, s57, s61\n" \
    \
  "s_waitcnt vmcnt(8)\n" \
  "s_waitcnt lgkmcnt(0)\n" \
  "s_barrier\n" \
  "ds_read_b128 v[32:35], v224 offset:32768\n" \
  "ds_read_b128 v[36:39], v225 offset:34816\n" \
  "ds_read_b128 v[40:43], v225 offset:32768\n" \
  "ds_read_b128 v[44:47], v224 offset:34816\n" \
    \
  "s_sleep 6\n" \
  "s_add_u32 m0, s60, 131072\n" \
  "s_nop 0\n" \
  "buffer_load_dwordx4 v236, s[48:51], s58 offen offset:0 lds\n" \
  "buffer_load_dwordx4 v237, s[48:51], s58 offen offset:1024 lds\n" \
  "buffer_load_dwordx4 v238, s[48:51], s58 offen offset:2048 lds\n" \
  "s_add_u32 s58, s58, 196608\n" \
  "s_add_u32 m0, s59, 0\n" \
  "s_nop 0\n" \
  "buffer_load_dwordx4 v216, s[40:43], s56 offen offset:0 lds\n" \
  "buffer_load_dwordx4 v217, s[40:43], s56 offen offset:1024 lds\n" \
  "buffer_load_dwordx4 v218, s[40:43], s56 offen offset:2048 lds\n" \
  "buffer_load_dwordx4 v219, s[40:43], s56 offen offset:3072 lds\n" \
  "s_add_u32 s56, s56, 128\n" \
  "s_add_u32 m0, s59, 65536\n" \
  "s_nop 0\n" \
  "buffer_load_dwordx4 v220, s[44:47], s57 offen offset:0 lds\n" \
  "buffer_load_dwordx4 v221, s[44:47], s57 offen offset:1024 lds\n" \
  "buffer_load_dwordx4 v222, s[44:47], s57 offen offset:2048 lds\n" \
  "buffer_load_dwordx4 v223, s[44:47], s57 offen offset:3072 lds\n" \
  "s_add_u32 s57, s57, s61\n" \
    \
  "s_waitcnt vmcnt(11)\n" \
  "s_waitcnt lgkmcnt(0)\n" \
  "s_barrier\n" \
  "ds_read_b128 v[48:51], v224 offset:49152\n" \
  "ds_read_b128 v[52:55], v225 offset:51200\n" \
  "ds_read_b128 v[56:59], v225 offset:49152\n" \
  "ds_read_b128 v[60:63], v224 offset:51200\n" \
  "ds_read_b64 v[246:247], v251 offset:64\n" \
  "ds_read_b128 v[160:163], v248 offset:0\n" \
  "ds_read_b128 v[168:171], v248 offset:64\n" \
  "ds_read_b128 v[176:179], v248 offset:128\n" \
  "ds_read_b128 v[184:187], v248 offset:192\n" \
  "ds_read_b128 v[192:195], v248 offset:256\n" \
  "ds_read_b128 v[200:203], v248 offset:320\n" \
  "s_add_u32 m0, s59, 16384\n" \
  "s_nop 0\n" \
  "buffer_load_dwordx4 v216, s[40:43], s56 offen offset:0 lds\n" \
  "buffer_load_dwordx4 v217, s[40:43], s56 offen offset:1024 lds\n" \
  "buffer_load_dwordx4 v218, s[40:43], s56 offen offset:2048 lds\n" \
  "buffer_load_dwordx4 v219, s[40:43], s56 offen offset:3072 lds\n" \
  "s_add_u32 s56, s56, 128\n" \
  "s_add_u32 m0, s59, 81920\n" \
  "s_nop 0\n" \
  "buffer_load_dwordx4 v220, s[44:47], s57 offen offset:0 lds\n" \
  "buffer_load_dwordx4 v221, s[44:47], s57 offen offset:1024 lds\n" \
  "buffer_load_dwordx4 v222, s[44:47], s57 offen offset:2048 lds\n" \
  "buffer_load_dwordx4 v223, s[44:47], s57 offen offset:3072 lds\n" \
  "s_add_u32 s57, s57, s61\n" \
    \
  "s_waitcnt vmcnt(8)\n" \
  "s_waitcnt lgkmcnt(0)\n" \
  "v_readfirstlane_b32 s62, v246\n" \
  "v_readfirstlane_b32 s63, v247\n" \
  "s_barrier\n" \
  "ds_read_b128 v[64:67], v224 offset:0\n" \
  "ds_read_b128 v[68:71], v225 offset:2048\n" \
  "ds_read_b128 v[72:75], v225 offset:0\n" \
  "ds_read_b128 v[76:79], v224 offset:2048\n" \
  "s_add_u32 m0, s59, 32768\n" \
  "s_nop 0\n" \
  "buffer_load_dwordx4 v216, s[40:43], s56 offen offset:0 lds\n" \
  "buffer_load_dwordx4 v217, s[40:43], s56 offen offset:1024 lds\n" \
  "buffer_load_dwordx4 v218, s[40:43], s56 offen offset:2048 lds\n" \
  "buffer_load_dwordx4 v219, s[40:43], s56 offen offset:3072 lds\n" \
  "s_add_u32 s56, s56, 128\n" \
  "s_add_u32 m0, s59, 98304\n" \
  "s_nop 0\n" \
  "buffer_load_dwordx4 v220, s[44:47], s57 offen offset:0 lds\n" \
  "buffer_load_dwordx4 v221, s[44:47], s57 offen offset:1024 lds\n" \
  "buffer_load_dwordx4 v222, s[44:47], s57 offen offset:2048 lds\n" \
  "buffer_load_dwordx4 v223, s[44:47], s57 offen offset:3072 lds\n" \
  "s_add_u32 s57, s57, s61\n" \
    \
  "s_waitcnt vmcnt(8)\n" \
  "s_waitcnt lgkmcnt(0)\n" \
  "s_barrier\n" \
  "ds_read_b128 v[80:83], v224 offset:16384\n" \
  "ds_read_b128 v[84:87], v225 offset:18432\n" \
  "ds_read_b128 v[88:91], v225 offset:16384\n" \
  "ds_read_b128 v[92:95], v224 offset:18432\n" \
  "s_add_u32 m0, s59, 49152\n" \
  "s_nop 0\n" \
  "buffer_load_dwordx4 v216, s[40:43], s56 offen offset:0 lds\n" \
  "buffer_load_dwordx4 v217, s[40:43], s56 offen offset:1024 lds\n" \
  "buffer_load_dwordx4 v218, s[40:43], s56 offen offset:2048 lds\n" \
  "buffer_load_dwordx4 v219, s[40:43], s56 offen offset:3072 lds\n" \
  "s_add_u32 s56, s56, 128\n" \
  "s_add_u32 m0, s59, 114688\n" \
  "s_nop 0\n" \
  "buffer_load_dwordx4 v220, s[44:47], s57 offen offset:0 lds\n" \
  "buffer_load_dwordx4 v221, s[44:47], s57 offen offset:1024 lds\n" \
  "buffer_load_dwordx4 v222, s[44:47], s57 offen offset:2048 lds\n" \
  "buffer_load_dwordx4 v223, s[44:47], s57 offen offset:3072 lds\n" \
  "s_add_u32 s57, s57, s61\n" \
    \
  "s_waitcnt vmcnt(8)\n" \
  "s_waitcnt lgkmcnt(0)\n" \
  "s_barrier\n" \
  "ds_read_b128 v[96:99], v224 offset:32768\n" \
  "ds_read_b128 v[100:103], v225 offset:34816\n" \
  "ds_read_b128 v[104:107], v225 offset:32768\n" \
  "ds_read_b128 v[108:111], v224 offset:34816\n" \
  "s_add_u32 m0, s59, 0\n" \
  "s_nop 0\n" \
  "buffer_load_dwordx4 v216, s[40:43], s56 offen offset:0 lds\n" \
  "buffer_load_dwordx4 v217, s[40:43], s56 offen offset:1024 lds\n" \
  "buffer_load_dwordx4 v218, s[40:43], s56 offen offset:2048 lds\n" \
  "buffer_load_dwordx4 v219, s[40:43], s56 offen offset:3072 lds\n" \
  "s_add_u32 s56, s56, 128\n" \
  "s_add_u32 m0, s59, 65536\n" \
  "s_nop 0\n" \
  "buffer_load_dwordx4 v220, s[44:47], s57 offen offset:0 lds\n" \
  "buffer_load_dwordx4 v221, s[44:47], s57 offen offset:1024 lds\n" \
  "buffer_load_dwordx4 v222, s[44:47], s57 offen offset:2048 lds\n" \
  "buffer_load_dwordx4 v223, s[44:47], s57 offen offset:3072 lds\n" \
  "s_add_u32 s57, s57, s61\n" \
    \
  "s_waitcnt vmcnt(8)\n" \
  "s_waitcnt lgkmcnt(0)\n" \
  "s_barrier\n" \
  "ds_read_b128 v[112:115], v224 offset:49152\n" \
  "ds_read_b128 v[116:119], v225 offset:51200\n" \
  "ds_read_b128 v[120:123], v225 offset:49152\n" \
  "ds_read_b128 v[124:127], v224 offset:51200\n" \
  "s_add_u32 m0, s59, 16384\n" \
  "s_nop 0\n" \
  "buffer_load_dwordx4 v216, s[40:43], s56 offen offset:0 lds\n" \
  "buffer_load_dwordx4 v217, s[40:43], s56 offen offset:1024 lds\n" \
  "buffer_load_dwordx4 v218, s[40:43], s56 offen offset:2048 lds\n" \
  "buffer_load_dwordx4 v219, s[40:43], s56 offen offset:3072 lds\n" \
  "s_add_u32 s56, s56, 128\n" \
  "s_add_u32 m0, s59, 81920\n" \
  "s_nop 0\n" \
  "buffer_load_dwordx4 v220, s[44:47], s57 offen offset:0 lds\n" \
  "buffer_load_dwordx4 v221, s[44:47], s57 offen offset:1024 lds\n" \
  "buffer_load_dwordx4 v222, s[44:47], s57 offen offset:2048 lds\n" \
  "buffer_load_dwordx4 v223, s[44:47], s57 offen offset:3072 lds\n" \
  "s_add_u32 s57, s57, s61\n" \
  "buffer_load_dwordx4 v[228:231], v226, s[52:55], s68 offen sc1\n" \
    \
  "s_waitcnt vmcnt(9)\n" \
  "s_waitcnt lgkmcnt(0)\n" \
  "s_barrier\n" \
  "ds_read_b128 v[128:131], v224 offset:0\n" \
  "ds_read_b128 v[132:135], v225 offset:2048\n" \
  "ds_read_b128 v[136:139], v225 offset:0\n" \
  "ds_read_b128 v[140:143], v224 offset:2048\n" \
  "s_add_u32 m0, s60, 143360\n" \
  "s_nop 0\n" \
  "buffer_load_dwordx4 v236, s[48:51], s58 offen offset:0 lds\n" \
  "buffer_load_dwordx4 v237, s[48:51], s58 offen offset:1024 lds\n" \
  "buffer_load_dwordx4 v238, s[48:51], s58 offen offset:2048 lds\n" \
  "s_add_u32 s58, s58, 196608\n" \
  "s_add_u32 m0, s60, 32768\n" \
  "s_nop 0\n" \
  "buffer_load_dwordx4 v236, s[48:51], s58 offen offset:0 lds\n" \
  "buffer_load_dwordx4 v237, s[48:51], s58 offen offset:1024 lds\n" \
  "buffer_load_dwordx4 v238, s[48:51], s58 offen offset:2048 lds\n" \
  "s_add_u32 s58, s58, 196608\n" \
    \
  "s_waitcnt vmcnt(7)\n" \
  "s_waitcnt lgkmcnt(0)\n" \
  "s_barrier\n" \
  "ds_read_b128 v[144:147], v224 offset:16384\n" \
  "ds_read_b128 v[148:151], v225 offset:18432\n" \
  "ds_read_b128 v[152:155], v225 offset:16384\n" \
  "ds_read_b128 v[156:159], v224 offset:18432\n" \
  "s_add_u32 m0, s60, 98304\n" \
  "s_nop 0\n" \
  "buffer_load_dwordx4 v236, s[48:51], s58 offen offset:0 lds\n" \
  "buffer_load_dwordx4 v237, s[48:51], s58 offen offset:1024 lds\n" \
  "buffer_load_dwordx4 v238, s[48:51], s58 offen offset:2048 lds\n" \
  "s_add_u32 s58, s58, 196608\n" \
    \
  "s_waitcnt vmcnt(9)\n" \
  "s_mov_b32 s64, 1048576\n" \
  "3:\n" \
  "v_cmp_ne_u32 vcc, s62, v229\n" \
  "v_cmp_ne_u32 s[66:67], s63, v231\n" \
  "s_or_b64 vcc, vcc, s[66:67]\n" \
  "s_cbranch_scc0 4f\n" \
  "s_sleep 1\n" \
  "buffer_load_dwordx4 v[228:231], v226, s[52:55], s68 offen sc1\n" \
  "s_sub_u32 s64, s64, 1\n" \
  "s_waitcnt vmcnt(0)\n" \
  "s_cbranch_scc0 3b\n" \
  "4:\n" \
  "v_mul_f32 v229, s65, v228\n" \
  "ds_write2st64_b32 v227, v228, v230 offset0:0 offset1:2\n" \
  "ds_write_b32 v227, v229 offset:1024\n" \
  "s_add_u32 m0, s60, 45056\n" \
  "s_nop 0\n" \
  "buffer_load_dwordx4 v236, s[48:51], s58 offen offset:0 lds\n" \
  "buffer_load_dwordx4 v237, s[48:51], s58 offen offset:1024 lds\n" \
  "buffer_load_dwordx4 v238, s[48:51], s58 offen offset:2048 lds\n" \
  "s_add_u32 s58, s58, 196608\n" \
    \
  "s_waitcnt lgkmcnt(0)\n" \
  "s_barrier\n" \
  "ds_read2_b32 v[232:233], v239 offset0:0 offset1:16\n" \
  "ds_read2_b32 v[208:209], v239 offset0:128 offset1:144\n" \
    \
  "s_setprio 0\n" \
  "s_waitcnt lgkmcnt(0)\n" \
  "v_rcp_f32 v233, v233\n" \
  "v_rcp_f32 v232, v232\n" \
  "v_mul_f32 v164, v233, v160\n" \
  "v_mul_f32 v165, v233, v161\n" \
  "v_mul_f32 v166, v233, v162\n" \
  "v_mul_f32 v167, v233, v163\n" \
  "v_mul_f32 v172, v233, v168\n" \
  "v_mul_f32 v173, v233, v169\n" \
  "v_mul_f32 v174, v233, v170\n" \
  "v_mul_f32 v175, v233, v171\n" \
  "v_mul_f32 v180, v233, v176\n" \
  "v_mul_f32 v181, v233, v177\n" \
  "v_mul_f32 v182, v233, v178\n" \
  "v_mul_f32 v183, v233, v179\n" \
  "v_mul_f32 v188, v233, v184\n" \
  "v_mul_f32 v189, v233, v185\n" \
  "v_mul_f32 v190, v233, v186\n" \
  "v_mul_f32 v191, v233, v187\n" \
  "v_mul_f32 v196, v233, v192\n" \
  "v_mul_f32 v197, v233, v193\n" \
  "v_mul_f32 v198, v233, v194\n" \
  "v_mul_f32 v199, v233, v195\n" \
  "v_mul_f32 v204, v233, v200\n" \
  "v_mul_f32 v205, v233, v201\n" \
  "v_mul_f32 v206, v233, v202\n" \
  "v_mul_f32 v207, v233, v203\n" \
  "v_mul_f32 v160, v232, v160\n" \
  "v_mul_f32 v161, v232, v161\n" \
  "v_mul_f32 v162, v232, v162\n" \
  "v_mul_f32 v163, v232, v163\n" \
  "v_mul_f32 v168, v232, v168\n" \
  "v_mul_f32 v169, v232, v169\n" \
  "v_mul_f32 v170, v232, v170\n" \
  "v_mul_f32 v171, v232, v171\n" \
  "v_mul_f32 v176, v232, v176\n" \
  "v_mul_f32 v177, v232, v177\n" \
  "v_mul_f32 v178, v232, v178\n" \
  "v_mul_f32 v179, v232, v179\n" \
  "v_mul_f32 v184, v232, v184\n" \
  "v_mul_f32 v185, v232, v185\n" \
  "v_mul_f32 v186, v232, v186\n" \
  "v_mul_f32 v187, v232, v187\n" \
  "v_mul_f32 v192, v232, v192\n" \
  "v_mul_f32 v193, v232, v193\n" \
  "v_mul_f32 v194, v232, v194\n" \
  "v_mul_f32 v195, v232, v195\n" \
  "v_mul_f32 v200, v232, v200\n" \
  "v_mul_f32 v201, v232, v201\n" \
  "v_mul_f32 v202, v232, v202\n" \
  "v_mul_f32 v203, v232, v203\n" \
  "s_waitcnt vmcnt(0)\n" \
  "s_barrier\n" \
  "s_setprio 2\n" \
    \
  "ds_read_b64_tr_b16 v[216:217], v244 offset:0\n" \
  "ds_read_b64_tr_b16 v[218:219], v244 offset:768\n" \
  "ds_read_b64_tr_b16 v[220:221], v245 offset:32\n" \
  "ds_read_b64_tr_b16 v[222:223], v245 offset:800\n" \
  "ds_read_b64_tr_b16 v[224:225], v244 offset:64\n" \
  "ds_read_b64_tr_b16 v[226:227], v244 offset:832\n" \
  "ds_read_b64_tr_b16 v[228:229], v245 offset:96\n" \
  "ds_read_b64_tr_b16 v[230:231], v245 offset:864\n" \
  "ds_read_b64_tr_b16 v[232:233], v244 offset:128\n" \
  "ds_read_b64_tr_b16 v[234:235], v244 offset:896\n" \
  "ds_read_b64_tr_b16 v[236:237], v245 offset:160\n" \
  "ds_read_b64_tr_b16 v[238:239], v245 offset:928\n" \
  "s_waitcnt lgkmcnt(6)\n" \
  "v_mfma_f32_16x16x32_bf16 v[160:163], v[216:219], v[0:3], v[160:163]\n" \
  "v_mfma_f32_16x16x32_bf16 v[164:167], v[216:219], v[4:7], v[164:167]\n" \
  "ds_read_b64_tr_b16 v[216:217], v244 offset:6144\n" \
  "v_mfma_f32_16x16x32_bf16 v[168:171], v[220:223], v[0:3], v[168:171]\n" \
  "ds_read_b64_tr_b16 v[218:219], v244 offset:6912\n" \
  "v_mfma_f32_16x16x32_bf16 v[172:175], v[220:223], v[4:7], v[172:175]\n" \
  "ds_read_b64_tr_b16 v[220:221], v245 offset:6176\n" \
  "v_mfma_f32_16x16x32_bf16 v[176:179], v[224:227], v[0:3], v[176:179]\n" \
  "ds_read_b64_tr_b16 v[222:223], v245 offset:6944\n" \
  "v_mfma_f32_16x16x32_bf16 v[180:183], v[224:227], v[4:7], v[180:183]\n" \
  "ds_read_b64_tr_b16 v[224:225], v244 offset:6208\n" \
  "s_waitcnt lgkmcnt(5)\n" \
  "v_mfma_f32_16x16x32_bf16 v[184:187], v[228:231], v[0:3], v[184:187]\n" \
  "ds_read_b64_tr_b16 v[226:227], v244 offset:6976\n" \
  "v_mfma_f32_16x16x32_bf16 v[188:191], v[228:231], v[4:7], v[188:191]\n" \
  "ds_read_b64_tr_b16 v[228:229], v245 offset:6240\n" \
  "v_mfma_f32_16x16x32_bf16 v[192:195], v[232:235], v[0:3], v[192:195]\n" \
  "ds_read_b64_tr_b16 v[230:231], v245 offset:7008\n" \
  "v_mfma_f32_16x16x32_bf16 v[196:199], v[232:235], v[4:7], v[196:199]\n" \
  "ds_read_b64_tr_b16 v[232:233], v244 offset:6272\n" \
  "v_mfma_f32_16x16x32_bf16 v[200:203], v[236:239], v[0:3], v[200:203]\n" \
  "ds_read_b64_tr_b16 v[234:235], v244 offset:7040\n" \
  "v_mfma_f32_16x16x32_bf16 v[204:207], v[236:239], v[4:7], v[204:207]\n" \
  "ds_read_b64_tr_b16 v[236:237], v245 offset:6304\n" \
  "ds_read_b64_tr_b16 v[238:239], v245 offset:7072\n" \
  "s_waitcnt lgkmcnt(6)\n" \
  "v_mfma_f32_16x16x32_bf16 v[160:163], v[216:219], v[8:11], v[160:163]\n" \
  "v_mfma_f32_16x16x32_bf16 v[164:167], v[216:219], v[12:15], v[164:167]\n" \
  "ds_read_b64_tr_b16 v[216:217], v244 offset:12288\n" \
  "v_mfma_f32_16x16x32_bf16 v[168:171], v[220:223], v[8:11], v[168:171]\n" \
  "ds_read_b64_tr_b16 v[218:219], v244 offset:13056\n" \
  "v_mfma_f32_16x16x32_bf16 v[172:175], v[220:223], v[12:15], v[172:175]\n" \
  "ds_read_b64_tr_b16 v[220:221], v245 offset:12320\n" \
  "v_mfma_f32_16x16x32_bf16 v[176:179], v[224:227], v[8:11], v[176:179]\n" \
  "ds_read_b64_tr_b16 v[222:223], v245 offset:13088\n" \
  "v_mfma_f32_16x16x32_bf16 v[180:183], v[224:227], v[12:15], v[180:183]\n" \
  "ds_read_b64_tr_b16 v[224:225], v244 offset:12352\n" \
  "s_waitcnt lgkmcnt(5)\n" \
  "v_mfma_f32_16x16x32_bf16 v[184:187], v[228:231], v[8:11], v[184:187]\n" \
  "ds_read_b64_tr_b16 v[226:227], v244 offset:13120\n" \
  "v_mfma_f32_16x16x32_bf16 v[188:191], v[228:231], v[12:15], v[188:191]\n" \
  "ds_read_b64_tr_b16 v[228:229], v245 offset:12384\n" \
  "v_mfma_f32_16x16x32_bf16 v[192:195], v[232:235], v[8:11], v[192:195]\n" \
  "ds_read_b64_tr_b16 v[230:231], v245 offset:13152\n" \
  "v_mfma_f32_16x16x32_bf16 v[196:199], v[232:235], v[12:15], v[196:199]\n" \
  "ds_read_b64_tr_b16 v[232:233], v244 offset:12416\n" \
  "v_mfma_f32_16x16x32_bf16 v[200:203], v[236:239], v[8:11], v[200:203]\n" \
  "ds_read_b64_tr_b16 v[234:235], v244 offset:13184\n" \
  "v_mfma_f32_16x16x32_bf16 v[204:207], v[236:239], v[12:15], v[204:207]\n" \
  "ds_read_b64_tr_b16 v[236:237], v245 offset:12448\n" \
  "ds_read_b64_tr_b16 v[238:239], v245 offset:13216\n" \
  "s_waitcnt lgkmcnt(6)\n" \
  "v_mfma_f32_16x16x32_bf16 v[160:163], v[216:219], v[16:19], v[160:163]\n" \
  "v_mfma_f32_16x16x32_bf16 v[164:167], v[216:219], v[20:23], v[164:167]\n" \
  "ds_read_b64_tr_b16 v[216:217], v244 offset:18432\n" \
  "v_mfma_f32_16x16x32_bf16 v[168:171], v[220:223], v[16:19], v[168:171]\n" \
  "ds_read_b64_tr_b16 v[218:219], v244 offset:19200\n" \
  "v_mfma_f32_16x16x32_bf16 v[172:175], v[220:223], v[20:23], v[172:175]\n" \
  "ds_read_b64_tr_b16 v[220:221], v245 offset:18464\n" \
  "v_mfma_f32_16x16x32_bf16 v[176:179], v[224:227], v[16:19], v[176:179]\n" \
  "ds_read_b64_tr_b16 v[222:223], v245 offset:19232\n" \
  "v_mfma_f32_16x16x32_bf16 v[180:183], v[224:227], v[20:23], v[180:183]\n" \
  "ds_read_b64_tr_b16 v[224:225], v244 offset:18496\n" \
  "s_waitcnt lgkmcnt(5)\n" \
  "v_mfma_f32_16x16x32_bf16 v[184:187], v[228:231], v[16:19], v[184:187]\n" \
  "ds_read_b64_tr_b16 v[226:227], v244 offset:19264\n" \
  "v_mfma_f32_16x16x32_bf16 v[188:191], v[228:231], v[20:23], v[188:191]\n" \
  "ds_read_b64_tr_b16 v[228:229], v245 offset:18528\n" \
  "v_mfma_f32_16x16x32_bf16 v[192:195], v[232:235], v[16:19], v[192:195]\n" \
  "ds_read_b64_tr_b16 v[230:231], v245 offset:19296\n" \
  "v_mfma_f32_16x16x32_bf16 v[196:199], v[232:235], v[20:23], v[196:199]\n" \
  "ds_read_b64_tr_b16 v[232:233], v244 offset:18560\n" \
  "v_mfma_f32_16x16x32_bf16 v[200:203], v[236:239], v[16:19], v[200:203]\n" \
  "ds_read_b64_tr_b16 v[234:235], v244 offset:19328\n" \
  "v_mfma_f32_16x16x32_bf16 v[204:207], v[236:239], v[20:23], v[204:207]\n" \
  "ds_read_b64_tr_b16 v[236:237], v245 offset:18592\n" \
  "ds_read_b64_tr_b16 v[238:239], v245 offset:19360\n" \
  "s_waitcnt lgkmcnt(6)\n" \
  "v_mfma_f32_16x16x32_bf16 v[160:163], v[216:219], v[24:27], v[160:163]\n" \
  "ds_read_b64_tr_b16 v[0:1], v240 offset:32768\n" \
  "v_mfma_f32_16x16x32_bf16 v[164:167], v[216:219], v[28:31], v[164:167]\n" \
  "ds_read_b64_tr_b16 v[2:3], v240 offset:33536\n" \
  "v_mfma_f32_16x16x32_bf16 v[168:171], v[220:223], v[24:27], v[168:171]\n" \
  "ds_read_b64_tr_b16 v[4:5], v241 offset:32800\n" \
  "v_mfma_f32_16x16x32_bf16 v[172:175], v[220:223], v[28:31], v[172:175]\n" \
  "ds_read_b64_tr_b16 v[6:7], v241 offset:33568\n" \
  "v_mfma_f32_16x16x32_bf16 v[176:179], v[224:227], v[24:27], v[176:179]\n" \
  "ds_read_b64_tr_b16 v[8:9], v240 offset:32832\n" \
  "v_mfma_f32_16x16x32_bf16 v[180:183], v[224:227], v[28:31], v[180:183]\n" \
  "ds_read_b64_tr_b16 v[10:11], v240 offset:33600\n" \
  "s_waitcnt lgkmcnt(6)\n" \
  "v_mfma_f32_16x16x32_bf16 v[184:187], v[228:231], v[24:27], v[184:187]\n" \
  "ds_read_b64_tr_b16 v[12:13], v241 offset:32864\n" \
  "v_mfma_f32_16x16x32_bf16 v[188:191], v[228:231], v[28:31], v[188:191]\n" \
  "ds_read_b64_tr_b16 v[14:15], v241 offset:33632\n" \
  "v_mfma_f32_16x16x32_bf16 v[192:195], v[232:235], v[24:27], v[192:195]\n" \
  "ds_read_b64_tr_b16 v[16:17], v240 offset:32896\n" \
  "v_mfma_f32_16x16x32_bf16 v[196:199], v[232:235], v[28:31], v[196:199]\n" \
  "ds_read_b64_tr_b16 v[18:19], v240 offset:33664\n" \
  "v_mfma_f32_16x16x32_bf16 v[200:203], v[236:239], v[24:27], v[200:203]\n" \
  "ds_read_b64_tr_b16 v[20:21], v241 offset:32928\n" \
  "v_mfma_f32_16x16x32_bf16 v[204:207], v[236:239], v[28:31], v[204:207]\n" \
  "ds_read_b64_tr_b16 v[22:23], v241 offset:33696\n" \
  "s_waitcnt lgkmcnt(6)\n" \
  "v_mfma_f32_16x16x32_bf16 v[160:163], v[0:3], v[32:35], v[160:163]\n" \
  "ds_read_b64_tr_b16 v[216:217], v240 offset:38912\n" \
  "v_mfma_f32_16x16x32_bf16 v[164:167], v[0:3], v[36:39], v[164:167]\n" \
  "ds_read_b64_tr_b16 v[218:219], v240 offset:39680\n" \
  "v_mfma_f32_16x16x32_bf16 v[168:171], v[4:7], v[32:35], v[168:171]\n" \
  "ds_read_b64_tr_b16 v[220:221], v241 offset:38944\n" \
  "v_mfma_f32_16x16x32_bf16 v[172:175], v[4:7], v[36:39], v[172:175]\n" \
  "ds_read_b64_tr_b16 v[222:223], v241 offset:39712\n" \
  "v_mfma_f32_16x16x32_bf16 v[176:179], v[8:11], v[32:35], v[176:179]\n" \
  "ds_read_b64_tr_b16 v[224:225], v240 offset:38976\n" \
  "v_mfma_f32_16x16x32_bf16 v[180:183], v[8:11], v[36:39], v[180:183]\n" \
  "ds_read_b64_tr_b16 v[226:227], v240 offset:39744\n" \
  "s_waitcnt lgkmcnt(6)\n" \
  "v_mfma_f32_16x16x32_bf16 v[184:187], v[12:15], v[32:35], v[184:187]\n" \
  "ds_read_b64_tr_b16 v[228:229], v241 offset:39008\n" \
  "v_mfma_f32_16x16x32_bf16 v[188:191], v[12:15], v[36:39], v[188:191]\n" \
  "ds_read_b64_tr_b16 v[230:231], v241 offset:39776\n" \
  "v_mfma_f32_16x16x32_bf16 v[192:195], v[16:19], v[32:35], v[192:195]\n" \
  "ds_read_b64_tr_b16 v[232:233], v240 offset:39040\n" \
  "v_mfma_f32_16x16x32_bf16 v[196:199], v[16:19], v[36:39], v[196:199]\n" \
  "ds_read_b64_tr_b16 v[234:235], v240 offset:39808\n" \
  "v_mfma_f32_16x16x32_bf16 v[200:203], v[20:23], v[32:35], v[200:203]\n" \
  "ds_read_b64_tr_b16 v[236:237], v241 offset:39072\n" \
  "v_mfma_f32_16x16x32_bf16 v[204:207], v[20:23], v[36:39], v[204:207]\n" \
  "ds_read_b64_tr_b16 v[238:239], v241 offset:39840\n" \
  "s_waitcnt lgkmcnt(6)\n" \
  "v_mfma_f32_16x16x32_bf16 v[160:163], v[216:219], v[40:43], v[160:163]\n" \
  "ds_read_b64_tr_b16 v[0:1], v242 offset:32768\n" \
  "v_mfma_f32_16x16x32_bf16 v[164:167], v[216:219], v[44:47], v[164:167]\n" \
  "ds_read_b64_tr_b16 v[2:3], v242 offset:33536\n" \
  "v_mfma_f32_16x16x32_bf16 v[168:171], v[220:223], v[40:43], v[168:171]\n" \
  "ds_read_b64_tr_b16 v[4:5], v243 offset:32800\n" \
  "v_mfma_f32_16x16x32_bf16 v[172:175], v[220:223], v[44:47], v[172:175]\n" \
  "ds_read_b64_tr_b16 v[6:7], v243 offset:33568\n" \
  "v_mfma_f32_16x16x32_bf16 v[176:179], v[224:227], v[40:43], v[176:179]\n" \
  "ds_read_b64_tr_b16 v[8:9], v242 offset:32832\n" \
  "v_mfma_f32_16x16x32_bf16 v[180:183], v[224:227], v[44:47], v[180:183]\n" \
  "ds_read_b64_tr_b16 v[10:11], v242 offset:33600\n" \
  "s_waitcnt lgkmcnt(6)\n" \
  "v_mfma_f32_16x16x32_bf16 v[184:187], v[228:231], v[40:43], v[184:187]\n" \
  "ds_read_b64_tr_b16 v[12:13], v243 offset:32864\n" \
  "v_mfma_f32_16x16x32_bf16 v[188:191], v[228:231], v[44:47], v[188:191]\n" \
  "ds_read_b64_tr_b16 v[14:15], v243 offset:33632\n" \
  "v_mfma_f32_16x16x32_bf16 v[192:195], v[232:235], v[40:43], v[192:195]\n" \
  "ds_read_b64_tr_b16 v[16:17], v242 offset:32896\n" \
  "v_mfma_f32_16x16x32_bf16 v[196:199], v[232:235], v[44:47], v[196:199]\n" \
  "ds_read_b64_tr_b16 v[18:19], v242 offset:33664\n" \
  "v_mfma_f32_16x16x32_bf16 v[200:203], v[236:239], v[40:43], v[200:203]\n" \
  "ds_read_b64_tr_b16 v[20:21], v243 offset:32928\n" \
  "v_mfma_f32_16x16x32_bf16 v[204:207], v[236:239], v[44:47], v[204:207]\n" \
  "ds_read_b64_tr_b16 v[22:23], v243 offset:33696\n" \
  "s_waitcnt lgkmcnt(6)\n" \
  "v_mfma_f32_16x16x32_bf16 v[160:163], v[0:3], v[48:51], v[160:163]\n" \
  "ds_read_b64_tr_b16 v[216:217], v242 offset:38912\n" \
  "v_mfma_f32_16x16x32_bf16 v[164:167], v[0:3], v[52:55], v[164:167]\n" \
  "ds_read_b64_tr_b16 v[218:219], v242 offset:39680\n" \
  "v_mfma_f32_16x16x32_bf16 v[168:171], v[4:7], v[48:51], v[168:171]\n" \
  "ds_read_b64_tr_b16 v[220:221], v243 offset:38944\n" \
  "v_mfma_f32_16x16x32_bf16 v[172:175], v[4:7], v[52:55], v[172:175]\n" \
  "ds_read_b64_tr_b16 v[222:223], v243 offset:39712\n" \
  "v_mfma_f32_16x16x32_bf16 v[176:179], v[8:11], v[48:51], v[176:179]\n" \
  "ds_read_b64_tr_b16 v[224:225], v242 offset:38976\n" \
  "v_mfma_f32_16x16x32_bf16 v[180:183], v[8:11], v[52:55], v[180:183]\n" \
  "ds_read_b64_tr_b16 v[226:227], v242 offset:39744\n" \
  "s_waitcnt lgkmcnt(6)\n" \
  "v_mfma_f32_16x16x32_bf16 v[184:187], v[12:15], v[48:51], v[184:187]\n" \
  "ds_read_b64_tr_b16 v[228:229], v243 offset:39008\n" \
  "v_mfma_f32_16x16x32_bf16 v[188:191], v[12:15], v[52:55], v[188:191]\n" \
  "ds_read_b64_tr_b16 v[230:231], v243 offset:39776\n" \
  "v_mfma_f32_16x16x32_bf16 v[192:195], v[16:19], v[48:51], v[192:195]\n" \
  "ds_read_b64_tr_b16 v[232:233], v242 offset:39040\n" \
  "v_mfma_f32_16x16x32_bf16 v[196:199], v[16:19], v[52:55], v[196:199]\n" \
  "ds_read_b64_tr_b16 v[234:235], v242 offset:39808\n" \
  "v_mfma_f32_16x16x32_bf16 v[200:203], v[20:23], v[48:51], v[200:203]\n" \
  "ds_read_b64_tr_b16 v[236:237], v243 offset:39072\n" \
  "v_mfma_f32_16x16x32_bf16 v[204:207], v[20:23], v[52:55], v[204:207]\n" \
  "ds_read_b64_tr_b16 v[238:239], v243 offset:39840\n" \
  "s_waitcnt lgkmcnt(6)\n" \
  "v_mfma_f32_16x16x32_bf16 v[160:163], v[216:219], v[56:59], v[160:163]\n" \
  "ds_read_b64_tr_b16 v[0:1], v240 offset:45056\n" \
  "v_mfma_f32_16x16x32_bf16 v[164:167], v[216:219], v[60:63], v[164:167]\n" \
  "ds_read_b64_tr_b16 v[2:3], v240 offset:45824\n" \
  "v_mfma_f32_16x16x32_bf16 v[168:171], v[220:223], v[56:59], v[168:171]\n" \
  "ds_read_b64_tr_b16 v[4:5], v241 offset:45088\n" \
  "v_mfma_f32_16x16x32_bf16 v[172:175], v[220:223], v[60:63], v[172:175]\n" \
  "ds_read_b64_tr_b16 v[6:7], v241 offset:45856\n" \
  "v_mfma_f32_16x16x32_bf16 v[176:179], v[224:227], v[56:59], v[176:179]\n" \
  "ds_read_b64_tr_b16 v[8:9], v240 offset:45120\n" \
  "v_mfma_f32_16x16x32_bf16 v[180:183], v[224:227], v[60:63], v[180:183]\n" \
  "ds_read_b64_tr_b16 v[10:11], v240 offset:45888\n" \
  "s_waitcnt lgkmcnt(6)\n" \
  "v_mfma_f32_16x16x32_bf16 v[184:187], v[228:231], v[56:59], v[184:187]\n" \
  "ds_read_b64_tr_b16 v[12:13], v241 offset:45152\n" \
  "v_mfma_f32_16x16x32_bf16 v[188:191], v[228:231], v[60:63], v[188:191]\n" \
  "ds_read_b64_tr_b16 v[14:15], v241 offset:45920\n" \
  "v_mfma_f32_16x16x32_bf16 v[192:195], v[232:235], v[56:59], v[192:195]\n" \
  "ds_read_b64_tr_b16 v[16:17], v240 offset:45184\n" \
  "v_mfma_f32_16x16x32_bf16 v[196:199], v[232:235], v[60:63], v[196:199]\n" \
  "ds_read_b64_tr_b16 v[18:19], v240 offset:45952\n" \
  "v_mfma_f32_16x16x32_bf16 v[200:203], v[236:239], v[56:59], v[200:203]\n" \
  "ds_read_b64_tr_b16 v[20:21], v241 offset:45216\n" \
  "v_mfma_f32_16x16x32_bf16 v[204:207], v[236:239], v[60:63], v[204:207]\n" \
  "ds_read_b64_tr_b16 v[22:23], v241 offset:45984\n" \
  "s_waitcnt lgkmcnt(6)\n" \
  "v_mfma_f32_16x16x32_bf16 v[160:163], v[0:3], v[64:67], v[160:163]\n" \
  "ds_read_b64_tr_b16 v[216:217], v240 offset:51200\n" \
  "v_mfma_f32_16x16x32_bf16 v[164:167], v[0:3], v[68:71], v[164:167]\n" \
  "ds_read_b64_tr_b16 v[218:219], v240 offset:51968\n" \
  "v_mfma_f32_16x16x32_bf16 v[168:171], v[4:7], v[64:67], v[168:171]\n" \
  "ds_read_b64_tr_b16 v[220:221], v241 offset:51232\n" \
  "v_mfma_f32_16x16x32_bf16 v[172:175], v[4:7], v[68:71], v[172:175]\n" \
  "ds_read_b64_tr_b16 v[222:223], v241 offset:52000\n" \
  "v_mfma_f32_16x16x32_bf16 v[176:179], v[8:11], v[64:67], v[176:179]\n" \
  "ds_read_b64_tr_b16 v[224:225], v240 offset:51264\n" \
  "v_mfma_f32_16x16x32_bf16 v[180:183], v[8:11], v[68:71], v[180:183]\n" \
  "ds_read_b64_tr_b16 v[226:227], v240 offset:52032\n" \
  "s_waitcnt lgkmcnt(6)\n" \
  "ds_read_b128 v[246:249], v251 offset:16\n" \
  "v_mfma_f32_16x16x32_bf16 v[184:187], v[12:15], v[64:67], v[184:187]\n" \
  "ds_read_b64_tr_b16 v[228:229], v241 offset:51296\n" \
  "v_mfma_f32_16x16x32_bf16 v[188:191], v[12:15], v[68:71], v[188:191]\n" \
  "ds_read_b64_tr_b16 v[230:231], v241 offset:52064\n" \
  "v_mfma_f32_16x16x32_bf16 v[192:195], v[16:19], v[64:67], v[192:195]\n" \
  "ds_read_b64_tr_b16 v[232:233], v240 offset:51328\n" \
  "v_mfma_f32_16x16x32_bf16 v[196:199], v[16:19], v[68:71], v[196:199]\n" \
  "ds_read_b64_tr_b16 v[234:235], v240 offset:52096\n" \
  "v_mfma_f32_16x16x32_bf16 v[200:203], v[20:23], v[64:67], v[200:203]\n" \
  "ds_read_b64_tr_b16 v[236:237], v241 offset:51360\n" \
  "v_mfma_f32_16x16x32_bf16 v[204:207], v[20:23], v[68:71], v[204:207]\n" \
  "ds_read_b64_tr_b16 v[238:239], v241 offset:52128\n" \
  "s_mov_b32 s64, 1048576\n" \
  "s_waitcnt lgkmcnt(6)\n" \
  "5:\n" \
  "v_min3_u32 v250, v246, v247, v248\n" \
  "v_min_u32 v250, v250, v249\n" \
  "v_cmp_gt_u32 vcc, 1, v250\n" \
  "s_cbranch_vccz 6f\n" \
  "ds_read_b128 v[246:249], v251 offset:16\n" \
  "s_sub_u32 s64, s64, 1\n" \
  "s_waitcnt lgkmcnt(0)\n" \
  "s_cbranch_scc0 5b\n" \
  "6:\n" \
  "v_mfma_f32_16x16x32_bf16 v[160:163], v[216:219], v[72:75], v[160:163]\n" \
  "ds_read_b64_tr_b16 v[0:1], v242 offset:45056\n" \
  "v_mfma_f32_16x16x32_bf16 v[164:167], v[216:219], v[76:79], v[164:167]\n" \
  "ds_read_b64_tr_b16 v[2:3], v242 offset:45824\n" \
  "v_mfma_f32_16x16x32_bf16 v[168:171], v[220:223], v[72:75], v[168:171]\n" \
  "ds_read_b64_tr_b16 v[4:5], v243 offset:45088\n" \
  "v_mfma_f32_16x16x32_bf16 v[172:175], v[220:223], v[76:79], v[172:175]\n" \
  "ds_read_b64_tr_b16 v[6:7], v243 offset:45856\n" \
  "v_mfma_f32_16x16x32_bf16 v[176:179], v[224:227], v[72:75], v[176:179]\n" \
  "ds_read_b64_tr_b16 v[8:9], v242 offset:45120\n" \
  "v_mfma_f32_16x16x32_bf16 v[180:183], v[224:227], v[76:79], v[180:183]\n" \
  "ds_read_b64_tr_b16 v[10:11], v242 offset:45888\n" \
  "s_waitcnt lgkmcnt(6)\n" \
  "v_mfma_f32_16x16x32_bf16 v[184:187], v[228:231], v[72:75], v[184:187]\n" \
  "ds_read_b64_tr_b16 v[12:13], v243 offset:45152\n" \
  "v_mfma_f32_16x16x32_bf16 v[188:191], v[228:231], v[76:79], v[188:191]\n" \
  "ds_read_b64_tr_b16 v[14:15], v243 offset:45920\n" \
  "v_mfma_f32_16x16x32_bf16 v[192:195], v[232:235], v[72:75], v[192:195]\n" \
  "ds_read_b64_tr_b16 v[16:17], v242 offset:45184\n" \
  "v_mfma_f32_16x16x32_bf16 v[196:199], v[232:235], v[76:79], v[196:199]\n" \
  "ds_read_b64_tr_b16 v[18:19], v242 offset:45952\n" \
  "v_mfma_f32_16x16x32_bf16 v[200:203], v[236:239], v[72:75], v[200:203]\n" \
  "ds_read_b64_tr_b16 v[20:21], v243 offset:45216\n" \
  "v_mfma_f32_16x16x32_bf16 v[204:207], v[236:239], v[76:79], v[204:207]\n" \
  "ds_read_b64_tr_b16 v[22:23], v243 offset:45984\n" \
  "s_waitcnt lgkmcnt(6)\n" \
  "v_mfma_f32_16x16x32_bf16 v[160:163], v[0:3], v[80:83], v[160:163]\n" \
  "ds_read_b64_tr_b16 v[216:217], v242 offset:51200\n" \
  "v_mfma_f32_16x16x32_bf16 v[164:167], v[0:3], v[84:87], v[164:167]\n" \
  "ds_read_b64_tr_b16 v[218:219], v242 offset:51968\n" \
  "v_mfma_f32_16x16x32_bf16 v[168:171], v[4:7], v[80:83], v[168:171]\n" \
  "ds_read_b64_tr_b16 v[220:221], v243 offset:51232\n" \
  "v_mfma_f32_16x16x32_bf16 v[172:175], v[4:7], v[84:87], v[172:175]\n" \
  "ds_read_b64_tr_b16 v[222:223], v243 offset:52000\n" \
  "v_mfma_f32_16x16x32_bf16 v[176:179], v[8:11], v[80:83], v[176:179]\n" \
  "ds_read_b64_tr_b16 v[224:225], v242 offset:51264\n" \
  "v_mfma_f32_16x16x32_bf16 v[180:183], v[8:11], v[84:87], v[180:183]\n" \
  "ds_read_b64_tr_b16 v[226:227], v242 offset:52032\n" \
  "s_waitcnt lgkmcnt(6)\n" \
  "v_mfma_f32_16x16x32_bf16 v[184:187], v[12:15], v[80:83], v[184:187]\n" \
  "ds_read_b64_tr_b16 v[228:229], v243 offset:51296\n" \
  "v_mfma_f32_16x16x32_bf16 v[188:191], v[12:15], v[84:87], v[188:191]\n" \
  "ds_read_b64_tr_b16 v[230:231], v243 offset:52064\n" \
  "v_mfma_f32_16x16x32_bf16 v[192:195], v[16:19], v[80:83], v[192:195]\n" \
  "ds_read_b64_tr_b16 v[232:233], v242 offset:51328\n" \
  "v_mfma_f32_16x16x32_bf16 v[196:199], v[16:19], v[84:87], v[196:199]\n" \
  "ds_read_b64_tr_b16 v[234:235], v242 offset:52096\n" \
  "v_mfma_f32_16x16x32_bf16 v[200:203], v[20:23], v[80:83], v[200:203]\n" \
  "ds_read_b64_tr_b16 v[236:237], v243 offset:51360\n" \
  "v_mfma_f32_16x16x32_bf16 v[204:207], v[20:23], v[84:87], v[204:207]\n" \
  "ds_read_b64_tr_b16 v[238:239], v243 offset:52128\n" \
  "s_waitcnt lgkmcnt(6)\n" \
  "v_mfma_f32_16x16x32_bf16 v[160:163], v[216:219], v[88:91], v[160:163]\n" \
  "ds_read_b64_tr_b16 v[0:1], v240 offset:0\n" \
  "v_mfma_f32_16x16x32_bf16 v[164:167], v[216:219], v[92:95], v[164:167]\n" \
  "ds_read_b64_tr_b16 v[2:3], v240 offset:768\n" \
  "v_mfma_f32_16x16x32_bf16 v[168:171], v[220:223], v[88:91], v[168:171]\n" \
  "ds_read_b64_tr_b16 v[4:5], v241 offset:32\n" \
  "v_mfma_f32_16x16x32_bf16 v[172:175], v[220:223], v[92:95], v[172:175]\n" \
  "ds_read_b64_tr_b16 v[6:7], v241 offset:800\n" \
  "v_mfma_f32_16x16x32_bf16 v[176:179], v[224:227], v[88:91], v[176:179]\n" \
  "ds_read_b64_tr_b16 v[8:9], v240 offset:64\n" \
  "v_mfma_f32_16x16x32_bf16 v[180:183], v[224:227], v[92:95], v[180:183]\n" \
  "ds_read_b64_tr_b16 v[10:11], v240 offset:832\n" \
  "s_waitcnt lgkmcnt(6)\n" \
  "v_mfma_f32_16x16x32_bf16 v[184:187], v[228:231], v[88:91], v[184:187]\n" \
  "ds_read_b64_tr_b16 v[12:13], v241 offset:96\n" \
  "v_mfma_f32_16x16x32_bf16 v[188:191], v[228:231], v[92:95], v[188:191]\n" \
  "ds_read_b64_tr_b16 v[14:15], v241 offset:864\n" \
  "v_mfma_f32_16x16x32_bf16 v[192:195], v[232:235], v[88:91], v[192:195]\n" \
  "ds_read_b64_tr_b16 v[16:17], v240 offset:128\n" \
  "v_mfma_f32_16x16x32_bf16 v[196:199], v[232:235], v[92:95], v[196:199]\n" \
  "ds_read_b64_tr_b16 v[18:19], v240 offset:896\n" \
  "v_mfma_f32_16x16x32_bf16 v[200:203], v[236:239], v[88:91], v[200:203]\n" \
  "ds_read_b64_tr_b16 v[20:21], v241 offset:160\n" \
  "v_mfma_f32_16x16x32_bf16 v[204:207], v[236:239], v[92:95], v[204:207]\n" \
  "ds_read_b64_tr_b16 v[22:23], v241 offset:928\n" \
  "buffer_load_dwordx4 v[24:27], v210, s[52:55], s69 offen sc1\n" \
  "s_waitcnt lgkmcnt(6)\n" \
  "v_mfma_f32_16x16x32_bf16 v[160:163], v[0:3], v[96:99], v[160:163]\n" \
  "ds_read_b64_tr_b16 v[216:217], v240 offset:6144\n" \
  "v_mfma_f32_16x16x32_bf16 v[164:167], v[0:3], v[100:103], v[164:167]\n" \
  "ds_read_b64_tr_b16 v[218:219], v240 offset:6912\n" \
  "v_mfma_f32_16x16x32_bf16 v[168:171], v[4:7], v[96:99], v[168:171]\n" \
  "ds_read_b64_tr_b16 v[220:221], v241 offset:6176\n" \
  "v_mfma_f32_16x16x32_bf16 v[172:175], v[4:7], v[100:103], v[172:175]\n" \
  "ds_read_b64_tr_b16 v[222:223], v241 offset:6944\n" \
  "v_mfma_f32_16x16x32_bf16 v[176:179], v[8:11], v[96:99], v[176:179]\n" \
  "ds_read_b64_tr_b16 v[224:225], v240 offset:6208\n" \
  "v_mfma_f32_16x16x32_bf16 v[180:183], v[8:11], v[100:103], v[180:183]\n" \
  "ds_read_b64_tr_b16 v[226:227], v240 offset:6976\n" \
  "s_waitcnt lgkmcnt(6)\n" \
  "ds_read_b128 v[246:249], v251 offset:16\n" \
  "v_mfma_f32_16x16x32_bf16 v[184:187], v[12:15], v[96:99], v[184:187]\n" \
  "ds_read_b64_tr_b16 v[228:229], v241 offset:6240\n" \
  "v_mfma_f32_16x16x32_bf16 v[188:191], v[12:15], v[100:103], v[188:191]\n" \
  "ds_read_b64_tr_b16 v[230:231], v241 offset:7008\n" \
  "v_mfma_f32_16x16x32_bf16 v[192:195], v[16:19], v[96:99], v[192:195]\n" \
  "ds_read_b64_tr_b16 v[232:233], v240 offset:6272\n" \
  "v_mfma_f32_16x16x32_bf16 v[196:199], v[16:19], v[100:103], v[196:199]\n" \
  "ds_read_b64_tr_b16 v[234:235], v240 offset:7040\n" \
  "v_mfma_f32_16x16x32_bf16 v[200:203], v[20:23], v[96:99], v[200:203]\n" \
  "ds_read_b64_tr_b16 v[236:237], v241 offset:6304\n" \
  "v_mfma_f32_16x16x32_bf16 v[204:207], v[20:23], v[100:103], v[204:207]\n" \
  "ds_read_b64_tr_b16 v[238:239], v241 offset:7072\n" \
  "s_mov_b32 s64, 1048576\n" \
  "s_waitcnt lgkmcnt(6)\n" \
  "5:\n" \
  "v_min3_u32 v250, v246, v247, v248\n" \
  "v_min_u32 v250, v250, v249\n" \
  "v_cmp_gt_u32 vcc, 2, v250\n" \
  "s_cbranch_vccz 6f\n" \
  "ds_read_b128 v[246:249], v251 offset:16\n" \
  "s_sub_u32 s64, s64, 1\n" \
  "s_waitcnt lgkmcnt(0)\n" \
  "s_cbranch_scc0 5b\n" \
  "6:\n" \
  "v_mfma_f32_16x16x32_bf16 v[160:163], v[216:219], v[104:107], v[160:163]\n" \
  "ds_read_b64_tr_b16 v[0:1], v242 offset:0\n" \
  "v_mfma_f32_16x16x32_bf16 v[164:167], v[216:219], v[108:111], v[164:167]\n" \
  "ds_read_b64_tr_b16 v[2:3], v242 offset:768\n" \
  "v_mfma_f32_16x16x32_bf16 v[168:171], v[220:223], v[104:107], v[168:171]\n" \
  "ds_read_b64_tr_b16 v[4:5], v243 offset:32\n" \
  "v_mfma_f32_16x16x32_bf16 v[172:175], v[220:223], v[108:111], v[172:175]\n" \
  "ds_read_b64_tr_b16 v[6:7], v243 offset:800\n" \
  "v_mfma_f32_16x16x32_bf16 v[176:179], v[224:227], v[104:107], v[176:179]\n" \
  "ds_read_b64_tr_b16 v[8:9], v242 offset:64\n" \
  "v_mfma_f32_16x16x32_bf16 v[180:183], v[224:227], v[108:111], v[180:183]\n" \
  "ds_read_b64_tr_b16 v[10:11], v242 offset:832\n" \
  "s_waitcnt lgkmcnt(6)\n" \
  "v_mfma_f32_16x16x32_bf16 v[184:187], v[228:231], v[104:107], v[184:187]\n" \
  "ds_read_b64_tr_b16 v[12:13], v243 offset:96\n" \
  "v_mfma_f32_16x16x32_bf16 v[188:191], v[228:231], v[108:111], v[188:191]\n" \
  "ds_read_b64_tr_b16 v[14:15], v243 offset:864\n" \
  "v_mfma_f32_16x16x32_bf16 v[192:195], v[232:235], v[104:107], v[192:195]\n" \
  "ds_read_b64_tr_b16 v[16:17], v242 offset:128\n" \
  "v_mfma_f32_16x16x32_bf16 v[196:199], v[232:235], v[108:111], v[196:199]\n" \
  "ds_read_b64_tr_b16 v[18:19], v242 offset:896\n" \
  "v_mfma_f32_16x16x32_bf16 v[200:203], v[236:239], v[104:107], v[200:203]\n" \
  "ds_read_b64_tr_b16 v[20:21], v243 offset:160\n" \
  "v_mfma_f32_16x16x32_bf16 v[204:207], v[236:239], v[108:111], v[204:207]\n" \
  "ds_read_b64_tr_b16 v[22:23], v243 offset:928\n" \
  "s_waitcnt lgkmcnt(6)\n" \
  "v_mfma_f32_16x16x32_bf16 v[160:163], v[0:3], v[112:115], v[160:163]\n" \
  "ds_read_b64_tr_b16 v[216:217], v242 offset:6144\n" \
  "v_mfma_f32_16x16x32_bf16 v[164:167], v[0:3], v[116:119], v[164:167]\n" \
  "ds_read_b64_tr_b16 v[218:219], v242 offset:6912\n" \
  "v_mfma_f32_16x16x32_bf16 v[168:171], v[4:7], v[112:115], v[168:171]\n" \
  "ds_read_b64_tr_b16 v[220:221], v243 offset:6176\n" \
  "v_mfma_f32_16x16x32_bf16 v[172:175], v[4:7], v[116:119], v[172:175]\n" \
  "ds_read_b64_tr_b16 v[222:223], v243 offset:6944\n" \
  "v_mfma_f32_16x16x32_bf16 v[176:179], v[8:11], v[112:115], v[176:179]\n" \
  "ds_read_b64_tr_b16 v[224:225], v242 offset:6208\n" \
  "v_mfma_f32_16x16x32_bf16 v[180:183], v[8:11], v[116:119], v[180:183]\n" \
  "ds_read_b64_tr_b16 v[226:227], v242 offset:6976\n" \
  "s_waitcnt lgkmcnt(6)\n" \
  "v_mfma_f32_16x16x32_bf16 v[184:187], v[12:15], v[112:115], v[184:187]\n" \
  "ds_read_b64_tr_b16 v[228:229], v243 offset:6240\n" \
  "v_mfma_f32_16x16x32_bf16 v[188:191], v[12:15], v[116:119], v[188:191]\n" \
  "ds_read_b64_tr_b16 v[230:231], v243 offset:7008\n" \
  "v_mfma_f32_16x16x32_bf16 v[192:195], v[16:19], v[112:115], v[192:195]\n" \
  "ds_read_b64_tr_b16 v[232:233], v242 offset:6272\n" \
  "v_mfma_f32_16x16x32_bf16 v[196:199], v[16:19], v[116:119], v[196:199]\n" \
  "ds_read_b64_tr_b16 v[234:235], v242 offset:7040\n" \
  "v_mfma_f32_16x16x32_bf16 v[200:203], v[20:23], v[112:115], v[200:203]\n" \
  "ds_read_b64_tr_b16 v[236:237], v243 offset:6304\n" \
  "v_mfma_f32_16x16x32_bf16 v[204:207], v[20:23], v[116:119], v[204:207]\n" \
  "ds_read_b64_tr_b16 v[238:239], v243 offset:7072\n" \
  "s_waitcnt lgkmcnt(6)\n" \
  "v_mfma_f32_16x16x32_bf16 v[160:163], v[216:219], v[120:123], v[160:163]\n" \
  "ds_read_b64_tr_b16 v[0:1], v240 offset:12288\n" \
  "v_mfma_f32_16x16x32_bf16 v[164:167], v[216:219], v[124:127], v[164:167]\n" \
  "ds_read_b64_tr_b16 v[2:3], v240 offset:13056\n" \
  "v_mfma_f32_16x16x32_bf16 v[168:171], v[220:223], v[120:123], v[168:171]\n" \
  "ds_read_b64_tr_b16 v[4:5], v241 offset:12320\n" \
  "v_mfma_f32_16x16x32_bf16 v[172:175], v[220:223], v[124:127], v[172:175]\n" \
  "ds_read_b64_tr_b16 v[6:7], v241 offset:13088\n" \
  "v_mfma_f32_16x16x32_bf16 v[176:179], v[224:227], v[120:123], v[176:179]\n" \
  "ds_read_b64_tr_b16 v[8:9], v240 offset:12352\n" \
  "v_mfma_f32_16x16x32_bf16 v[180:183], v[224:227], v[124:127], v[180:183]\n" \
  "ds_read_b64_tr_b16 v[10:11], v240 offset:13120\n" \
  "s_waitcnt lgkmcnt(6)\n" \
  "v_mfma_f32_16x16x32_bf16 v[184:187], v[228:231], v[120:123], v[184:187]\n" \
  "ds_read_b64_tr_b16 v[12:13], v241 offset:12384\n" \
  "v_mfma_f32_16x16x32_bf16 v[188:191], v[228:231], v[124:127], v[188:191]\n" \
  "ds_read_b64_tr_b16 v[14:15], v241 offset:13152\n" \
  "v_mfma_f32_16x16x32_bf16 v[192:195], v[232:235], v[120:123], v[192:195]\n" \
  "ds_read_b64_tr_b16 v[16:17], v240 offset:12416\n" \
  "v_mfma_f32_16x16x32_bf16 v[196:199], v[232:235], v[124:127], v[196:199]\n" \
  "ds_read_b64_tr_b16 v[18:19], v240 offset:13184\n" \
  "v_mfma_f32_16x16x32_bf16 v[200:203], v[236:239], v[120:123], v[200:203]\n" \
  "ds_read_b64_tr_b16 v[20:21], v241 offset:12448\n" \
  "v_mfma_f32_16x16x32_bf16 v[204:207], v[236:239], v[124:127], v[204:207]\n" \
  "ds_read_b64_tr_b16 v[22:23], v241 offset:13216\n" \
  "s_waitcnt lgkmcnt(6)\n" \
  "v_mfma_f32_16x16x32_bf16 v[160:163], v[0:3], v[128:131], v[160:163]\n" \
  "ds_read_b64_tr_b16 v[216:217], v240 offset:18432\n" \
  "v_mfma_f32_16x16x32_bf16 v[164:167], v[0:3], v[132:135], v[164:167]\n" \
  "ds_read_b64_tr_b16 v[218:219], v240 offset:19200\n" \
  "v_mfma_f32_16x16x32_bf16 v[168:171], v[4:7], v[128:131], v[168:171]\n" \
  "ds_read_b64_tr_b16 v[220:221], v241 offset:18464\n" \
  "v_mfma_f32_16x16x32_bf16 v[172:175], v[4:7], v[132:135], v[172:175]\n" \
  "ds_read_b64_tr_b16 v[222:223], v241 offset:19232\n" \
  "v_mfma_f32_16x16x32_bf16 v[176:179], v[8:11], v[128:131], v[176:179]\n" \
  "ds_read_b64_tr_b16 v[224:225], v240 offset:18496\n" \
  "v_mfma_f32_16x16x32_bf16 v[180:183], v[8:11], v[132:135], v[180:183]\n" \
  "ds_read_b64_tr_b16 v[226:227], v240 offset:19264\n" \
  "s_waitcnt lgkmcnt(6)\n" \
  "v_mfma_f32_16x16x32_bf16 v[184:187], v[12:15], v[128:131], v[184:187]\n" \
  "ds_read_b64_tr_b16 v[228:229], v241 offset:18528\n" \
  "v_mfma_f32_16x16x32_bf16 v[188:191], v[12:15], v[132:135], v[188:191]\n" \
  "ds_read_b64_tr_b16 v[230:231], v241 offset:19296\n" \
  "v_mfma_f32_16x16x32_bf16 v[192:195], v[16:19], v[128:131], v[192:195]\n" \
  "ds_read_b64_tr_b16 v[232:233], v240 offset:18560\n" \
  "v_mfma_f32_16x16x32_bf16 v[196:199], v[16:19], v[132:135], v[196:199]\n" \
  "ds_read_b64_tr_b16 v[234:235], v240 offset:19328\n" \
  "v_mfma_f32_16x16x32_bf16 v[200:203], v[20:23], v[128:131], v[200:203]\n" \
  "ds_read_b64_tr_b16 v[236:237], v241 offset:18592\n" \
  "v_mfma_f32_16x16x32_bf16 v[204:207], v[20:23], v[132:135], v[204:207]\n" \
  "ds_read_b64_tr_b16 v[238:239], v241 offset:19360\n" \
  "s_waitcnt lgkmcnt(6)\n" \
  "v_mfma_f32_16x16x32_bf16 v[160:163], v[216:219], v[136:139], v[160:163]\n" \
  "ds_read_b64_tr_b16 v[0:1], v242 offset:12288\n" \
  "v_mfma_f32_16x16x32_bf16 v[164:167], v[216:219], v[140:143], v[164:167]\n" \
  "ds_read_b64_tr_b16 v[2:3], v242 offset:13056\n" \
  "v_mfma_f32_16x16x32_bf16 v[168:171], v[220:223], v[136:139], v[168:171]\n" \
  "ds_read_b64_tr_b16 v[4:5], v243 offset:12320\n" \
  "v_mfma_f32_16x16x32_bf16 v[172:175], v[220:223], v[140:143], v[172:175]\n" \
  "ds_read_b64_tr_b16 v[6:7], v243 offset:13088\n" \
  "v_mfma_f32_16x16x32_bf16 v[176:179], v[224:227], v[136:139], v[176:179]\n" \
  "ds_read_b64_tr_b16 v[8:9], v242 offset:12352\n" \
  "v_mfma_f32_16x16x32_bf16 v[180:183], v[224:227], v[140:143], v[180:183]\n" \
  "ds_read_b64_tr_b16 v[10:11], v242 offset:13120\n" \
  "s_waitcnt lgkmcnt(6)\n" \
  "v_mfma_f32_16x16x32_bf16 v[184:187], v[228:231], v[136:139], v[184:187]\n" \
  "ds_read_b64_tr_b16 v[12:13], v243 offset:12384\n" \
  "v_mfma_f32_16x16x32_bf16 v[188:191], v[228:231], v[140:143], v[188:191]\n" \
  "ds_read_b64_tr_b16 v[14:15], v243 offset:13152\n" \
  "v_mfma_f32_16x16x32_bf16 v[192:195], v[232:235], v[136:139], v[192:195]\n" \
  "ds_read_b64_tr_b16 v[16:17], v242 offset:12416\n" \
  "v_mfma_f32_16x16x32_bf16 v[196:199], v[232:235], v[140:143], v[196:199]\n" \
  "ds_read_b64_tr_b16 v[18:19], v242 offset:13184\n" \
  "v_mfma_f32_16x16x32_bf16 v[200:203], v[236:239], v[136:139], v[200:203]\n" \
  "ds_read_b64_tr_b16 v[20:21], v243 offset:12448\n" \
  "v_mfma_f32_16x16x32_bf16 v[204:207], v[236:239], v[140:143], v[204:207]\n" \
  "ds_read_b64_tr_b16 v[22:23], v243 offset:13216\n" \
  "s_waitcnt vmcnt(0)\n" \
  "s_mov_b32 s64, 1048576\n" \
  "3:\n" \
  "v_cmp_ne_u32 vcc, s62, v25\n" \
  "v_cmp_ne_u32 s[66:67], s63, v27\n" \
  "s_or_b64 vcc, vcc, s[66:67]\n" \
  "s_cbranch_scc0 4f\n" \
  "buffer_load_dwordx4 v[24:27], v210, s[52:55], s69 offen sc1\n" \
  "s_sub_u32 s64, s64, 1\n" \
  "s_waitcnt vmcnt(0)\n" \
  "s_cbranch_scc0 3b\n" \
  "4:\n" \
  "ds_write2_b32 v211, v24, v26 offset0:0 offset1:1\n" \
  "s_waitcnt lgkmcnt(7)\n" \
  "v_mfma_f32_16x16x32_bf16 v[160:163], v[0:3], v[144:147], v[160:163]\n" \
  "ds_read_b64_tr_b16 v[216:217], v242 offset:18432\n" \
  "v_mfma_f32_16x16x32_bf16 v[164:167], v[0:3], v[148:151], v[164:167]\n" \
  "ds_read_b64_tr_b16 v[218:219], v242 offset:19200\n" \
  "v_mfma_f32_16x16x32_bf16 v[168:171], v[4:7], v[144:147], v[168:171]\n" \
  "ds_read_b64_tr_b16 v[220:221], v243 offset:18464\n" \
  "v_mfma_f32_16x16x32_bf16 v[172:175], v[4:7], v[148:151], v[172:175]\n" \
  "ds_read_b64_tr_b16 v[222:223], v243 offset:19232\n" \
  "v_mfma_f32_16x16x32_bf16 v[176:179], v[8:11], v[144:147], v[176:179]\n" \
  "ds_read_b64_tr_b16 v[224:225], v242 offset:18496\n" \
  "v_mfma_f32_16x16x32_bf16 v[180:183], v[8:11], v[148:151], v[180:183]\n" \
  "ds_read_b64_tr_b16 v[226:227], v242 offset:19264\n" \
  "s_waitcnt lgkmcnt(7)\n" \
  "v_mfma_f32_16x16x32_bf16 v[184:187], v[12:15], v[144:147], v[184:187]\n" \
  "ds_read_b64_tr_b16 v[228:229], v243 offset:18528\n" \
  "v_mfma_f32_16x16x32_bf16 v[188:191], v[12:15], v[148:151], v[188:191]\n" \
  "ds_read_b64_tr_b16 v[230:231], v243 offset:19296\n" \
  "v_mfma_f32_16x16x32_bf16 v[192:195], v[16:19], v[144:147], v[192:195]\n" \
  "ds_read_b64_tr_b16 v[232:233], v242 offset:18560\n" \
  "v_mfma_f32_16x16x32_bf16 v[196:199], v[16:19], v[148:151], v[196:199]\n" \
  "ds_read_b64_tr_b16 v[234:235], v242 offset:19328\n" \
  "v_mfma_f32_16x16x32_bf16 v[200:203], v[20:23], v[144:147], v[200:203]\n" \
  "ds_read_b64_tr_b16 v[236:237], v243 offset:18592\n" \
  "v_mfma_f32_16x16x32_bf16 v[204:207], v[20:23], v[148:151], v[204:207]\n" \
  "ds_read_b64_tr_b16 v[238:239], v243 offset:19360\n" \
  "s_waitcnt lgkmcnt(6)\n" \
  "v_mfma_f32_16x16x32_bf16 v[160:163], v[216:219], v[152:155], v[160:163]\n" \
  "ds_read_b128 v[24:27], v212 offset:0\n" \
  "v_mfma_f32_16x16x32_bf16 v[164:167], v[216:219], v[156:159], v[164:167]\n" \
  "ds_read_b128 v[28:31], v212 offset:64\n" \
  "v_mfma_f32_16x16x32_bf16 v[168:171], v[220:223], v[152:155], v[168:171]\n" \
  "ds_read_b128 v[32:35], v212 offset:128\n" \
  "v_mfma_f32_16x16x32_bf16 v[172:175], v[220:223], v[156:159], v[172:175]\n" \
  "ds_read_b128 v[36:39], v212 offset:192\n" \
  "v_mfma_f32_16x16x32_bf16 v[176:179], v[224:227], v[152:155], v[176:179]\n" \
  "ds_read_b128 v[40:43], v212 offset:256\n" \
  "v_mfma_f32_16x16x32_bf16 v[180:183], v[224:227], v[156:159], v[180:183]\n" \
  "ds_read_b128 v[44:47], v212 offset:320\n" \
  "s_waitcnt lgkmcnt(6)\n" \
  "v_mfma_f32_16x16x32_bf16 v[184:187], v[228:231], v[152:155], v[184:187]\n" \
  "v_mfma_f32_16x16x32_bf16 v[188:191], v[228:231], v[156:159], v[188:191]\n" \
  "v_mfma_f32_16x16x32_bf16 v[192:195], v[232:235], v[152:155], v[192:195]\n" \
  "v_mfma_f32_16x16x32_bf16 v[196:199], v[232:235], v[156:159], v[196:199]\n" \
  "v_mfma_f32_16x16x32_bf16 v[200:203], v[236:239], v[152:155], v[200:203]\n" \
  "v_mfma_f32_16x16x32_bf16 v[204:207], v[236:239], v[156:159], v[204:207]\n" \
  "global_load_dwordx4 v[228:231], v213, s[34:35] offset:0\n" \
  "global_load_dwordx4 v[232:235], v213, s[34:35] offset:1024\n" \
  "global_load_dwordx4 v[220:223], v213, s[72:73] offset:0\n" \
  "global_load_dwordx4 v[224:227], v213, s[72:73] offset:1024\n" \
  "s_waitcnt lgkmcnt(0)\n" \
    \
  "s_add_u32 s67, s70, 49152\n" \
  "v_fmac_f32 v160, v24, v208\n" \
  "v_fmac_f32 v161, v25, v208\n" \
  "v_fmac_f32 v162, v26, v208\n" \
  "v_fmac_f32 v163, v27, v208\n" \
  "v_fmac_f32 v168, v28, v208\n" \
  "v_fmac_f32 v169, v29, v208\n" \
  "v_fmac_f32 v170, v30, v208\n" \
  "v_fmac_f32 v171, v31, v208\n" \
  "v_max_f32 v160, 0, v160\n" \
  "v_max_f32 v161, 0, v161\n" \
  "v_max_f32 v162, 0, v162\n" \
  "v_max_f32 v163, 0, v163\n" \
  "v_max_f32 v168, 0, v168\n" \
  "v_max_f32 v169, 0, v169\n" \
  "v_max_f32 v170, 0, v170\n" \
  "v_max_f32 v171, 0, v171\n" \
  "v_mul_f32 v160, v160, v160\n" \
  "v_mul_f32 v161, v161, v161\n" \
  "v_mul_f32 v162, v162, v162\n" \
  "v_mul_f32 v163, v163, v163\n" \
  "v_mul_f32 v168, v168, v168\n" \
  "v_mul_f32 v169, v169, v169\n" \
  "v_mul_f32 v170, v170, v170\n" \
  "v_mul_f32 v171, v171, v171\n" \
  "v_cvt_pk_bf16_f32 v48, v160, v161\n" \
  "v_cvt_pk_bf16_f32 v49, v162, v163\n" \
  "v_cvt_pk_bf16_f32 v50, v168, v169\n" \
  "v_cvt_pk_bf16_f32 v51, v170, v171\n" \
  "buffer_store_dwordx4 v[48:51], v213, s[52:55], s70 offen offset:0 sc1\n" \
  "v_fmac_f32 v164, v24, v209\n" \
  "v_fmac_f32 v165, v25, v209\n" \
  "v_fmac_f32 v166, v26, v209\n" \
  "v_fmac_f32 v167, v27, v209\n" \
  "v_fmac_f32 v172, v28, v209\n" \
  "v_fmac_f32 v173, v29, v209\n" \
  "v_fmac_f32 v174, v30, v209\n" \
  "v_fmac_f32 v175, v31, v209\n" \
  "v_max_f32 v164, 0, v164\n" \
  "v_max_f32 v165, 0, v165\n" \
  "v_max_f32 v166, 0, v166\n" \
  "v_max_f32 v167, 0, v167\n" \
  "v_max_f32 v172, 0, v172\n" \
  "v_max_f32 v173, 0, v173\n" \
  "v_max_f32 v174, 0, v174\n" \
  "v_max_f32 v175, 0, v175\n" \
  "v_mul_f32 v164, v164, v164\n" \
  "v_mul_f32 v165, v165, v165\n" \
  "v_mul_f32 v166, v166, v166\n" \
  "v_mul_f32 v167, v167, v167\n" \
  "v_mul_f32 v172, v172, v172\n" \
  "v_mul_f32 v173, v173, v173\n" \
  "v_mul_f32 v174, v174, v174\n" \
  "v_mul_f32 v175, v175, v175\n" \
  "v_cvt_pk_bf16_f32 v52, v164, v165\n" \
  "v_cvt_pk_bf16_f32 v53, v166, v167\n" \
  "v_cvt_pk_bf16_f32 v54, v172, v173\n" \
  "v_cvt_pk_bf16_f32 v55, v174, v175\n" \
  "buffer_store_dwordx4 v[52:55], v213, s[52:55], s67 offen offset:0 sc1\n" \
  "v_fmac_f32 v176, v32, v208\n" \
  "v_fmac_f32 v177, v33, v208\n" \
  "v_fmac_f32 v178, v34, v208\n" \
  "v_fmac_f32 v179, v35, v208\n" \
  "v_fmac_f32 v184, v36, v208\n" \
  "v_fmac_f32 v185, v37, v208\n" \
  "v_fmac_f32 v186, v38, v208\n" \
  "v_fmac_f32 v187, v39, v208\n" \
  "v_max_f32 v176, 0, v176\n" \
  "v_max_f32 v177, 0, v177\n" \
  "v_max_f32 v178, 0, v178\n" \
  "v_max_f32 v179, 0, v179\n" \
  "v_max_f32 v184, 0, v184\n" \
  "v_max_f32 v185, 0, v185\n" \
  "v_max_f32 v186, 0, v186\n" \
  "v_max_f32 v187, 0, v187\n" \
  "v_mul_f32 v176, v176, v176\n" \
  "v_mul_f32 v177, v177, v177\n" \
  "v_mul_f32 v178, v178, v178\n" \
  "v_mul_f32 v179, v179, v179\n" \
  "v_mul_f32 v184, v184, v184\n" \
  "v_mul_f32 v185, v185, v185\n" \
  "v_mul_f32 v186, v186, v186\n" \
  "v_mul_f32 v187, v187, v187\n" \
  "v_cvt_pk_bf16_f32 v56, v176, v177\n" \
  "v_cvt_pk_bf16_f32 v57, v178, v179\n" \
  "v_cvt_pk_bf16_f32 v58, v184, v185\n" \
  "v_cvt_pk_bf16_f32 v59, v186, v187\n" \
  "buffer_store_dwordx4 v[56:59], v213, s[52:55], s70 offen offset:1024 sc1\n" \
  "v_fmac_f32 v180, v32, v209\n" \
  "v_fmac_f32 v181, v33, v209\n" \
  "v_fmac_f32 v182, v34, v209\n" \
  "v_fmac_f32 v183, v35, v209\n" \
  "v_fmac_f32 v188, v36, v209\n" \
  "v_fmac_f32 v189, v37, v209\n" \
  "v_fmac_f32 v190, v38, v209\n" \
  "v_fmac_f32 v191, v39, v209\n" \
  "v_max_f32 v180, 0, v180\n" \
  "v_max_f32 v181, 0, v181\n" \
  "v_max_f32 v182, 0, v182\n" \
  "v_max_f32 v183, 0, v183\n" \
  "v_max_f32 v188, 0, v188\n" \
  "v_max_f32 v189, 0, v189\n" \
  "v_max_f32 v190, 0, v190\n" \
  "v_max_f32 v191, 0, v191\n" \
  "v_mul_f32 v180, v180, v180\n" \
  "v_mul_f32 v181, v181, v181\n" \
  "v_mul_f32 v182, v182, v182\n" \
  "v_mul_f32 v183, v183, v183\n" \
  "v_mul_f32 v188, v188, v188\n" \
  "v_mul_f32 v189, v189, v189\n" \
  "v_mul_f32 v190, v190, v190\n" \
  "v_mul_f32 v191, v191, v191\n" \
  "v_cvt_pk_bf16_f32 v60, v180, v181\n" \
  "v_cvt_pk_bf16_f32 v61, v182, v183\n" \
  "v_cvt_pk_bf16_f32 v62, v188, v189\n" \
  "v_cvt_pk_bf16_f32 v63, v190, v191\n" \
  "buffer_store_dwordx4 v[60:63], v213, s[52:55], s67 offen offset:1024 sc1\n" \
  "v_fmac_f32 v192, v40, v208\n" \
  "v_fmac_f32 v193, v41, v208\n" \
  "v_fmac_f32 v194, v42, v208\n" \
  "v_fmac_f32 v195, v43, v208\n" \
  "v_fmac_f32 v200, v44, v208\n" \
  "v_fmac_f32 v201, v45, v208\n" \
  "v_fmac_f32 v202, v46, v208\n" \
  "v_fmac_f32 v203, v47, v208\n" \
  "v_max_f32 v192, 0, v192\n" \
  "v_max_f32 v193, 0, v193\n" \
  "v_max_f32 v194, 0, v194\n" \
  "v_max_f32 v195, 0, v195\n" \
  "v_max_f32 v200, 0, v200\n" \
  "v_max_f32 v201, 0, v201\n" \
  "v_max_f32 v202, 0, v202\n" \
  "v_max_f32 v203, 0, v203\n" \
  "v_mul_f32 v192, v192, v192\n" \
  "v_mul_f32 v193, v193, v193\n" \
  "v_mul_f32 v194, v194, v194\n" \
  "v_mul_f32 v195, v195, v195\n" \
  "v_mul_f32 v200, v200, v200\n" \
  "v_mul_f32 v201, v201, v201\n" \
  "v_mul_f32 v202, v202, v202\n" \
  "v_mul_f32 v203, v203, v203\n" \
  "v_cvt_pk_bf16_f32 v64, v192, v193\n" \
  "v_cvt_pk_bf16_f32 v65, v194, v195\n" \
  "v_cvt_pk_bf16_f32 v66, v200, v201\n" \
  "v_cvt_pk_bf16_f32 v67, v202, v203\n" \
  "buffer_store_dwordx4 v[64:67], v213, s[52:55], s70 offen offset:2048 sc1\n" \
  "v_fmac_f32 v196, v40, v209\n" \
  "v_fmac_f32 v197, v41, v209\n" \
  "v_fmac_f32 v198, v42, v209\n" \
  "v_fmac_f32 v199, v43, v209\n" \
  "v_fmac_f32 v204, v44, v209\n" \
  "v_fmac_f32 v205, v45, v209\n" \
  "v_fmac_f32 v206, v46, v209\n" \
  "v_fmac_f32 v207, v47, v209\n" \
  "v_max_f32 v196, 0, v196\n" \
  "v_max_f32 v197, 0, v197\n" \
  "v_max_f32 v198, 0, v198\n" \
  "v_max_f32 v199, 0, v199\n" \
  "v_max_f32 v204, 0, v204\n" \
  "v_max_f32 v205, 0, v205\n" \
  "v_max_f32 v206, 0, v206\n" \
  "v_max_f32 v207, 0, v207\n" \
  "v_mul_f32 v196, v196, v196\n" \
  "v_mul_f32 v197, v197, v197\n" \
  "v_mul_f32 v198, v198, v198\n" \
  "v_mul_f32 v199, v199, v199\n" \
  "v_mul_f32 v204, v204, v204\n" \
  "v_mul_f32 v205, v205, v205\n" \
  "v_mul_f32 v206, v206, v206\n" \
  "v_mul_f32 v207, v207, v207\n" \
  "v_cvt_pk_bf16_f32 v68, v196, v197\n" \
  "v_cvt_pk_bf16_f32 v69, v198, v199\n" \
  "v_cvt_pk_bf16_f32 v70, v204, v205\n" \
  "v_cvt_pk_bf16_f32 v71, v206, v207\n" \
  "buffer_store_dwordx4 v[68:71], v213, s[52:55], s67 offen offset:2048 sc1\n" \
    \
  "ds_read_b128 v[246:249], v251 offset:16\n" \
  "s_mov_b32 s64, 1048576\n" \
  "s_waitcnt lgkmcnt(0)\n" \
  "5:\n" \
  "v_min3_u32 v250, v246, v247, v248\n" \
  "v_min_u32 v250, v250, v249\n" \
  "v_cmp_gt_u32 vcc, 9, v250\n" \
  "s_cbranch_vccz 6f\n" \
  "ds_read_b128 v[246:249], v251 offset:16\n" \
  "s_sub_u32 s64, s64, 1\n" \
  "s_waitcnt lgkmcnt(0)\n" \
  "s_cbranch_scc0 5b\n" \
  "6:\n" \
  "s_waitcnt vmcnt(6)\n" \
  "s_add_u32 m0, s38, 0\n" \
  "s_nop 0\n" \
  "buffer_load_dwordx4 v220, s[76:79], s80 offen lds\n" \
  "s_sleep 1\n" \
  "s_add_u32 m0, s38, 1024\n" \
  "s_nop 0\n" \
  "buffer_load_dwordx4 v221, s[76:79], s80 offen lds\n" \
  "s_sleep 1\n" \
  "s_add_u32 m0, s38, 2048\n" \
  "s_nop 0\n" \
  "buffer_load_dwordx4 v222, s[76:79], s80 offen lds\n" \
  "s_sleep 1\n" \
  "s_add_u32 m0, s38, 3072\n" \
  "s_nop 0\n" \
  "buffer_load_dwordx4 v223, s[76:79], s80 offen lds\n" \
  "s_sleep 1\n" \
  "s_add_u32 m0, s38, 4096\n" \
  "s_nop 0\n" \
  "buffer_load_dwordx4 v224, s[76:79], s80 offen lds\n" \
  "s_sleep 1\n" \
  "s_mov_b32 s44, s71\n" \
    \
  "s_mov_b64 s[60:61], s[62:63]\n" \
  "s_and_b32 s27, s26, 7\n" \
  "s_bfe_u32 s34, s26, 0x20003\n" \
  "s_lshl2_add_u32 s27, s27, s34\n" \
  "s_lshr_b32 s33, s26, 5\n" \
  "s_mov_b32 s40, s24\n" \
  "s_and_b32 s41, s25, 0xffff\n" \
  "s_mov_b32 s42, 20152320\n" \
  "s_mov_b32 s43, 0x20000\n" \
  "s_mov_b32 s52, s8\n" \
  "s_and_b32 s53, s9, 0xffff\n" \
  "s_mov_b32 s54, 2621440\n" \
  "s_mov_b32 s55, 0x20000\n" \
  "s_mov_b32 s56, s22\n" \
  "s_and_b32 s57, s23, 0xffff\n" \
  "s_mov_b32 s58, 2621440\n" \
  "s_mov_b32 s59, 0x20000\n" \
  "s_mul_i32 s64, s33, 348160\n" \
  "s_mul_i32 s34, s29, 61440\n" \
  "s_add_u32 s64, s64, s34\n" \
  "s_add_u32 s64, s64, 17367040\n" \
  "s_mul_i32 s66, s33, 348160\n" \
  "s_lshl_b32 s34, s29, 10\n" \
  "s_add_u32 s66, s66, s34\n" \
  "s_add_u32 s66, s66, 17612800\n" \
  "s_lshl_b32 s67, s29, 10\n" \
  "s_mul_i32 s51, s33, 8704\n" \
  "s_add_u32 s51, s51, 458752\n" \
  "s_mul_i32 s45, s29, 1536\n" \
  "s_mul_i32 s33, s33, 160\n" \
  "s_add_u32 s62, s20, s33\n" \
  "s_addc_u32 s63, s21, 0\n" \
  "s_add_u32 s35, s29, 0\n" \
  "s_and_b32 s35, s35, 3\n" \
  "s_mul_i32 s68, s27, 196608\n" \
  "s_mul_i32 s34, s35, 49152\n" \
  "s_add_u32 s68, s68, s34\n" \
  "s_mul_i32 s34, s29, 12288\n" \
  "s_add_u32 s68, s68, s34\n" \
  "s_add_u32 s68, s68, 8454144\n" \
  "s_mul_i32 s72, s35, 15360\n" \
  "s_add_u32 s72, s72, 94208\n" \
  "s_add_u32 s35, s29, 1\n" \
  "s_and_b32 s35, s35, 3\n" \
  "s_mul_i32 s69, s27, 196608\n" \
  "s_mul_i32 s34, s35, 49152\n" \
  "s_add_u32 s69, s69, s34\n" \
  "s_mul_i32 s34, s29, 12288\n" \
  "s_add_u32 s69, s69, s34\n" \
  "s_add_u32 s69, s69, 8454144\n" \
  "s_mul_i32 s73, s35, 15360\n" \
  "s_add_u32 s73, s73, 94208\n" \
  "s_add_u32 s35, s29, 2\n" \
  "s_and_b32 s35, s35, 3\n" \
  "s_mul_i32 s70, s27, 196608\n" \
  "s_mul_i32 s34, s35, 49152\n" \
  "s_add_u32 s70, s70, s34\n" \
  "s_mul_i32 s34, s29, 12288\n" \
  "s_add_u32 s70, s70, s34\n" \
  "s_add_u32 s70, s70, 8454144\n" \
  "s_mul_i32 s74, s35, 15360\n" \
  "s_add_u32 s74, s74, 94208\n" \
  "s_add_u32 s35, s29, 3\n" \
  "s_and_b32 s35, s35, 3\n" \
  "s_mul_i32 s71, s27, 196608\n" \
  "s_mul_i32 s34, s35, 49152\n" \
  "s_add_u32 s71, s71, s34\n" \
  "s_mul_i32 s34, s29, 12288\n" \
  "s_add_u32 s71, s71, s34\n" \
  "s_add_u32 s71, s71, 8454144\n" \
  "s_mul_i32 s75, s35, 15360\n" \
  "s_add_u32 s75, s75, 94208\n" \
  "s_lshl_b32 s76, s27, 12\n" \
  "s_lshl_b32 s34, s29, 9\n" \
  "s_add_u32 s76, s76, s34\n" \
  "s_add_u32 s76, s76, 196608\n" \
  "s_lshl_b32 s77, s27, 12\n" \
  "s_add_u32 s77, s77, 327680\n" \
  "s_lshr_b32 s35, s29, 1\n" \
  "s_mul_i32 s78, s27, 81920\n" \
  "s_lshl_b32 s34, s35, 9\n" \
  "s_add_u32 s78, s78, s34\n" \
  "s_lshl_b32 s34, s29, 8\n" \
  "s_add_u32 s78, s78, s34\n" \
  "s_add_u32 s78, s78, 14745600\n" \
  "s_mul_i32 s79, s27, 81920\n" \
  "s_lshl_b32 s34, s29, 7\n" \
  "s_add_u32 s79, s79, s34\n" \
  "s_add_u32 s79, s79, 14745600\n" \
  "s_mul_i32 s80, s27, 81920\n" \
  "s_mul_i32 s34, s29, 20480\n" \
  "s_add_u32 s80, s80, s34\n" \
  "s_add_u32 s80, s80, s33\n" \
  "s_lshl_b32 s81, s27, 10\n" \
  "s_lshl_b32 s34, s29, 8\n" \
  "s_add_u32 s81, s81, s34\n" \
  "s_add_u32 s81, s81, 32768\n" \
  "s_lshl_b32 s82, s29, 2\n" \
  "s_add_u32 s82, s82, 157184\n" \
  "v_mov_b32 v247, 157184\n" \
  "v_mov_b32 v248, s82\n" \
  "s_mov_b64 exec, 0xff\n" \
  "v_add_u32 v232, s76, v232\n" \
  "s_not_b64 exec, exec\n" \
  "v_add_u32 v232, s51, v232\n" \
  "s_mov_b64 exec, 0xfff00\n" \
  "v_add_u32 v232, s45, v232\n" \
  "s_mov_b64 exec, -1\n" \
  "v_add_u32 v225, s38, v225\n" \
  "v_mov_b32 v20, s60\n" \
  "v_mov_b32 v21, s61\n" \
  "v_lshrrev_b32 v22, 1, v231\n" \
    \
  "s_waitcnt vmcnt(0)\n" \
  "ds_read_b64_tr_b16 v[0:1], v225 offset:0\n" \
  "ds_read_b64_tr_b16 v[2:3], v225 offset:2560\n" \
  "ds_read_b64_tr_b16 v[4:5], v225 offset:8\n" \
  "ds_read_b64_tr_b16 v[6:7], v225 offset:2568\n" \
  "ds_read_b64_tr_b16 v[8:9], v225 offset:16\n" \
  "ds_read_b64_tr_b16 v[10:11], v225 offset:2576\n" \
  "ds_read_b64_tr_b16 v[12:13], v225 offset:24\n" \
  "ds_read_b64_tr_b16 v[14:15], v225 offset:2584\n" \
  "ds_read_b64_tr_b16 v[16:17], v225 offset:32\n" \
  "ds_read_b64_tr_b16 v[18:19], v225 offset:2592\n" \
  "s_mov_b64 exec, 0xffff\n" \
  "buffer_store_dwordx2 v[20:21], v22, s[40:43], s44 offen sc1\n" \
  "s_mov_b64 exec, -1\n" \
  "s_waitcnt lgkmcnt(8)\n" \
  "buffer_store_dwordx4 v[0:3], v231, s[40:43], s36 offen offset:0 sc1\n" \
  "s_waitcnt lgkmcnt(6)\n" \
  "buffer_store_dwordx4 v[4:7], v231, s[40:43], s36 offen offset:1024 sc1\n" \
  "s_waitcnt lgkmcnt(4)\n" \
  "buffer_store_dwordx4 v[8:11], v231, s[40:43], s36 offen offset:2048 sc1\n" \
  "s_waitcnt lgkmcnt(2)\n" \
  "buffer_store_dwordx4 v[12:15], v231, s[40:43], s36 offen offset:3072 sc1\n" \
  "s_waitcnt lgkmcnt(0)\n" \
  "s_add_u32 s36, s36, 4096\n" \
  "buffer_store_dwordx4 v[16:19], v231, s[40:43], s36 offen offset:0 sc1\n" \
  "s_waitcnt vmcnt(0)\n" \
  "s_mov_b64 exec, 0xffff\n" \
  "buffer_store_dwordx2 v[20:21], v22, s[40:43], s37 offen sc1\n" \
  "s_mov_b64 exec, -1\n" \
  "v_mov_b32 v250, 3\n" \
  "s_waitcnt lgkmcnt(0)\n" \
  "ds_write_b32 v248, v250\n" \
  "s_mov_b32 s83, 1048576\n" \
  "5:\n" \
  "ds_read_b128 v[250:253], v247\n" \
  "s_waitcnt lgkmcnt(0)\n" \
  "v_min3_u32 v250, v250, v251, v252\n" \
  "v_min_u32 v250, v250, v253\n" \
  "v_cmp_gt_u32 vcc, 3, v250\n" \
  "s_cbranch_vccz 6f\n" \
  "s_sleep 2\n" \
  "s_sub_u32 s83, s83, 1\n" \
  "s_cbranch_scc0 5b\n" \
  "6:\n" \
    \
  "s_sleep 16\n" \
  "s_mov_b32 s83, 1048576\n" \
  "7:\n" \
  "buffer_load_dwordx2 v[252:253], v232, s[40:43], 0 offen sc1\n" \
  "s_waitcnt vmcnt(0)\n" \
  "v_cmp_ne_u64 vcc, s[60:61], v[252:253]\n" \
  "s_cbranch_vccz 8f\n" \
  "s_mov_b64 exec, vcc\n" \
  "s_sleep 8\n" \
  "s_sub_u32 s83, s83, 1\n" \
  "s_cbranch_scc0 7b\n" \
  "8:\n" \
  "s_mov_b64 exec, -1\n" \
  "s_setprio 0\n" \
    \
  "buffer_load_dwordx4 v[140:143], v231, s[40:43], s64 offen offset:0 sc1\n" \
  "s_sleep 1\n" \
  "buffer_load_dwordx4 v[144:147], v231, s[40:43], s64 offen offset:1024 sc1\n" \
  "s_sleep 1\n" \
  "buffer_load_dwordx4 v[148:151], v231, s[40:43], s64 offen offset:2048 sc1\n" \
  "s_sleep 1\n" \
  "buffer_load_dwordx4 v[152:155], v231, s[40:43], s64 offen offset:3072 sc1\n" \
  "s_add_u32 s64, s64, 4096\n" \
  "s_sleep 1\n" \
  "buffer_load_dwordx4 v[156:159], v231, s[40:43], s64 offen offset:0 sc1\n" \
  "s_add_u32 s64, s64, 1024\n" \
  "s_sleep 1\n" \
  "buffer_load_dwordx4 v[92:95], v231, s[40:43], s68 offen sc1\n" \
  "s_add_u32 s68, s68, 1024\n" \
  "s_sleep 1\n" \
  "buffer_load_dwordx4 v[96:99], v231, s[40:43], s69 offen sc1\n" \
  "s_add_u32 s69, s69, 1024\n" \
  "s_sleep 1\n" \
  "buffer_load_dwordx4 v[100:103], v231, s[40:43], s70 offen sc1\n" \
  "s_add_u32 s70, s70, 1024\n" \
  "s_sleep 1\n" \
  "buffer_load_dwordx4 v[104:107], v231, s[40:43], s71 offen sc1\n" \
  "s_add_u32 s71, s71, 1024\n" \
  "s_sleep 1\n" \
  "buffer_load_dwordx4 v[160:163], v231, s[40:43], s64 offen offset:0 sc1\n" \
  "s_sleep 1\n" \
  "buffer_load_dwordx4 v[164:167], v231, s[40:43], s64 offen offset:1024 sc1\n" \
  "s_sleep 1\n" \
  "buffer_load_dwordx4 v[168:171], v231, s[40:43], s64 offen offset:2048 sc1\n" \
  "s_sleep 1\n" \
  "buffer_load_dwordx4 v[172:175], v231, s[40:43], s64 offen offset:3072 sc1\n" \
  "s_add_u32 s64, s64, 4096\n" \
  "s_sleep 1\n" \
  "buffer_load_dwordx4 v[176:179], v231, s[40:43], s64 offen offset:0 sc1\n" \
  "s_add_u32 s64, s64, 1024\n" \
  "s_sleep 1\n" \
  "buffer_load_dwordx4 v[108:111], v231, s[40:43], s68 offen sc1\n" \
  "s_add_u32 s68, s68, 1024\n" \
  "s_sleep 1\n" \
  "buffer_load_dwordx4 v[112:115], v231, s[40:43], s69 offen sc1\n" \
  "s_add_u32 s69, s69, 1024\n" \
  "s_sleep 1\n" \
  "buffer_load_dwordx4 v[116:119], v231, s[40:43], s70 offen sc1\n" \
  "s_add_u32 s70, s70, 1024\n" \
  "s_sleep 1\n" \
  "buffer_load_dwordx4 v[120:123], v231, s[40:43], s71 offen sc1\n" \
  "s_add_u32 s71, s71, 1024\n" \
  "s_sleep 1\n" \
  "s_waitcnt vmcnt(9)\n" \
  "v_mfma_f32_16x16x32_bf16 v[0:3], v[140:143], v[92:95], 0\n" \
  "buffer_load_dwordx4 v[180:183], v231, s[40:43], s64 offen offset:0 sc1\n" \
  "s_setprio 2\n" \
  "v_mfma_f32_16x16x32_bf16 v[4:7], v[140:143], v[96:99], 0\n" \
  "s_setprio 0\n" \
  "v_mfma_f32_16x16x32_bf16 v[8:11], v[140:143], v[100:103], 0\n" \
  "buffer_load_dwordx4 v[184:187], v231, s[40:43], s64 offen offset:1024 sc1\n" \
  "v_mfma_f32_16x16x32_bf16 v[12:15], v[140:143], v[104:107], 0\n" \
  "v_mfma_f32_16x16x32_bf16 v[16:19], v[144:147], v[92:95], 0\n" \
  "buffer_load_dwordx4 v[188:191], v231, s[40:43], s64 offen offset:2048 sc1\n" \
  "v_mfma_f32_16x16x32_bf16 v[20:23], v[144:147], v[96:99], 0\n" \
  "v_mfma_f32_16x16x32_bf16 v[24:27], v[144:147], v[100:103], 0\n" \
  "buffer_load_dwordx4 v[192:195], v231, s[40:43], s64 offen offset:3072 sc1\n" \
  "s_add_u32 s64, s64, 4096\n" \
  "s_setprio 2\n" \
  "v_mfma_f32_16x16x32_bf16 v[28:31], v[144:147], v[104:107], 0\n" \
  "s_setprio 0\n" \
  "v_mfma_f32_16x16x32_bf16 v[32:35], v[148:151], v[92:95], 0\n" \
  "buffer_load_dwordx4 v[196:199], v231, s[40:43], s64 offen offset:0 sc1\n" \
  "s_add_u32 s64, s64, 1024\n" \
  "v_mfma_f32_16x16x32_bf16 v[36:39], v[148:151], v[96:99], 0\n" \
  "v_mfma_f32_16x16x32_bf16 v[40:43], v[148:151], v[100:103], 0\n" \
  "v_mfma_f32_16x16x32_bf16 v[44:47], v[148:151], v[104:107], 0\n" \
  "buffer_load_dwordx4 v[124:127], v231, s[40:43], s68 offen sc1\n" \
  "s_add_u32 s68, s68, 1024\n" \
  "v_mfma_f32_16x16x32_bf16 v[48:51], v[152:155], v[92:95], 0\n" \
  "s_setprio 2\n" \
  "v_mfma_f32_16x16x32_bf16 v[52:55], v[152:155], v[96:99], 0\n" \
  "s_setprio 0\n" \
  "buffer_load_dwordx4 v[128:131], v231, s[40:43], s69 offen sc1\n" \
  "s_add_u32 s69, s69, 1024\n" \
  "v_mfma_f32_16x16x32_bf16 v[56:59], v[152:155], v[100:103], 0\n" \
  "v_mfma_f32_16x16x32_bf16 v[60:63], v[152:155], v[104:107], 0\n" \
  "buffer_load_dwordx4 v[132:135], v231, s[40:43], s70 offen sc1\n" \
  "s_add_u32 s70, s70, 1024\n" \
  "v_mfma_f32_16x16x32_bf16 v[64:67], v[156:159], v[92:95], 0\n" \
  "v_mfma_f32_16x16x32_bf16 v[68:71], v[156:159], v[96:99], 0\n" \
  "buffer_load_dwordx4 v[136:139], v231, s[40:43], s71 offen sc1\n" \
  "s_add_u32 s71, s71, 1024\n" \
  "v_mfma_f32_16x16x32_bf16 v[72:75], v[156:159], v[100:103], 0\n" \
  "s_setprio 2\n" \
  "v_mfma_f32_16x16x32_bf16 v[76:79], v[156:159], v[104:107], 0\n" \
  "s_setprio 0\n" \
  "s_mov_b32 s39, 2\n" \
  "1:\n" \
  "s_waitcnt vmcnt(9)\n" \
  "v_mfma_f32_16x16x32_bf16 v[0:3], v[160:163], v[108:111], v[0:3]\n" \
  "buffer_load_dwordx4 v[140:143], v231, s[40:43], s64 offen offset:0 sc1\n" \
  "s_setprio 2\n" \
  "v_mfma_f32_16x16x32_bf16 v[4:7], v[160:163], v[112:115], v[4:7]\n" \
  "s_setprio 0\n" \
  "buffer_load_dwordx4 v[144:147], v231, s[40:43], s64 offen offset:1024 sc1\n" \
  "v_mfma_f32_16x16x32_bf16 v[8:11], v[160:163], v[116:119], v[8:11]\n" \
  "v_mfma_f32_16x16x32_bf16 v[12:15], v[160:163], v[120:123], v[12:15]\n" \
  "buffer_load_dwordx4 v[148:151], v231, s[40:43], s64 offen offset:2048 sc1\n" \
  "v_mfma_f32_16x16x32_bf16 v[16:19], v[164:167], v[108:111], v[16:19]\n" \
  "v_mfma_f32_16x16x32_bf16 v[20:23], v[164:167], v[112:115], v[20:23]\n" \
  "buffer_load_dwordx4 v[152:155], v231, s[40:43], s64 offen offset:3072 sc1\n" \
  "s_add_u32 s64, s64, 4096\n" \
  "v_mfma_f32_16x16x32_bf16 v[24:27], v[164:167], v[116:119], v[24:27]\n" \
  "s_setprio 2\n" \
  "v_mfma_f32_16x16x32_bf16 v[28:31], v[164:167], v[120:123], v[28:31]\n" \
  "s_setprio 0\n" \
  "buffer_load_dwordx4 v[156:159], v231, s[40:43], s64 offen offset:0 sc1\n" \
  "s_add_u32 s64, s64, 1024\n" \
  "v_mfma_f32_16x16x32_bf16 v[32:35], v[168:171], v[108:111], v[32:35]\n" \
  "v_mfma_f32_16x16x32_bf16 v[36:39], v[168:171], v[112:115], v[36:39]\n" \
  "buffer_load_dwordx4 v[92:95], v231, s[40:43], s68 offen sc1\n" \
  "s_add_u32 s68, s68, 1024\n" \
  "v_mfma_f32_16x16x32_bf16 v[40:43], v[168:171], v[116:119], v[40:43]\n" \
  "buffer_load_dwordx4 v[96:99], v231, s[40:43], s69 offen sc1\n" \
  "s_add_u32 s69, s69, 1024\n" \
  "v_mfma_f32_16x16x32_bf16 v[44:47], v[168:171], v[120:123], v[44:47]\n" \
  "v_mfma_f32_16x16x32_bf16 v[48:51], v[172:175], v[108:111], v[48:51]\n" \
  "buffer_load_dwordx4 v[100:103], v231, s[40:43], s70 offen sc1\n" \
  "s_add_u32 s70, s70, 1024\n" \
  "s_setprio 2\n" \
  "v_mfma_f32_16x16x32_bf16 v[52:55], v[172:175], v[112:115], v[52:55]\n" \
  "s_setprio 0\n" \
  "v_mfma_f32_16x16x32_bf16 v[56:59], v[172:175], v[116:119], v[56:59]\n" \
  "buffer_load_dwordx4 v[104:107], v231, s[40:43], s71 offen sc1\n" \
  "s_add_u32 s71, s71, 1024\n" \
  "v_mfma_f32_16x16x32_bf16 v[60:63], v[172:175], v[120:123], v[60:63]\n" \
  "v_mfma_f32_16x16x32_bf16 v[64:67], v[176:179], v[108:111], v[64:67]\n" \
  "s_mov_b32 m0, s67\n" \
  "s_add_u32 s67, s67, 4096\n" \
  "buffer_load_dwordx4 v231, s[40:43], s66 offen sc1 lds\n" \
  "s_add_u32 s66, s66, 4096\n" \
  "v_mfma_f32_16x16x32_bf16 v[68:71], v[176:179], v[112:115], v[68:71]\n" \
  "v_mfma_f32_16x16x32_bf16 v[72:75], v[176:179], v[116:119], v[72:75]\n" \
  "s_mov_b32 m0, s67\n" \
  "s_add_u32 s67, s67, 4096\n" \
  "buffer_load_dwordx4 v231, s[40:43], s66 offen sc1 lds\n" \
  "s_add_u32 s66, s66, 4096\n" \
  "s_setprio 2\n" \
  "v_mfma_f32_16x16x32_bf16 v[76:79], v[176:179], v[120:123], v[76:79]\n" \
  "s_setprio 0\n" \
  "s_waitcnt vmcnt(11)\n" \
  "v_mfma_f32_16x16x32_bf16 v[0:3], v[180:183], v[124:127], v[0:3]\n" \
  "buffer_load_dwordx4 v[160:163], v231, s[40:43], s64 offen offset:0 sc1\n" \
  "s_setprio 2\n" \
  "v_mfma_f32_16x16x32_bf16 v[4:7], v[180:183], v[128:131], v[4:7]\n" \
  "s_setprio 0\n" \
  "buffer_load_dwordx4 v[164:167], v231, s[40:43], s64 offen offset:1024 sc1\n" \
  "v_mfma_f32_16x16x32_bf16 v[8:11], v[180:183], v[132:135], v[8:11]\n" \
  "v_mfma_f32_16x16x32_bf16 v[12:15], v[180:183], v[136:139], v[12:15]\n" \
  "buffer_load_dwordx4 v[168:171], v231, s[40:43], s64 offen offset:2048 sc1\n" \
  "v_mfma_f32_16x16x32_bf16 v[16:19], v[184:187], v[124:127], v[16:19]\n" \
  "v_mfma_f32_16x16x32_bf16 v[20:23], v[184:187], v[128:131], v[20:23]\n" \
  "buffer_load_dwordx4 v[172:175], v231, s[40:43], s64 offen offset:3072 sc1\n" \
  "s_add_u32 s64, s64, 4096\n" \
  "v_mfma_f32_16x16x32_bf16 v[24:27], v[184:187], v[132:135], v[24:27]\n" \
  "s_setprio 2\n" \
  "v_mfma_f32_16x16x32_bf16 v[28:31], v[184:187], v[136:139], v[28:31]\n" \
  "s_setprio 0\n" \
  "buffer_load_dwordx4 v[176:179], v231, s[40:43], s64 offen offset:0 sc1\n" \
  "s_add_u32 s64, s64, 1024\n" \
  "v_mfma_f32_16x16x32_bf16 v[32:35], v[188:191], v[124:127], v[32:35]\n" \
  "v_mfma_f32_16x16x32_bf16 v[36:39], v[188:191], v[128:131], v[36:39]\n" \
  "buffer_load_dwordx4 v[108:111], v231, s[40:43], s68 offen sc1\n" \
  "s_add_u32 s68, s68, 1024\n" \
  "v_mfma_f32_16x16x32_bf16 v[40:43], v[188:191], v[132:135], v[40:43]\n" \
  "buffer_load_dwordx4 v[112:115], v231, s[40:43], s69 offen sc1\n" \
  "s_add_u32 s69, s69, 1024\n" \
  "v_mfma_f32_16x16x32_bf16 v[44:47], v[188:191], v[136:139], v[44:47]\n" \
  "v_mfma_f32_16x16x32_bf16 v[48:51], v[192:195], v[124:127], v[48:51]\n" \
  "buffer_load_dwordx4 v[116:119], v231, s[40:43], s70 offen sc1\n" \
  "s_add_u32 s70, s70, 1024\n" \
  "s_setprio 2\n" \
  "v_mfma_f32_16x16x32_bf16 v[52:55], v[192:195], v[128:131], v[52:55]\n" \
  "s_setprio 0\n" \
  "v_mfma_f32_16x16x32_bf16 v[56:59], v[192:195], v[132:135], v[56:59]\n" \
  "buffer_load_dwordx4 v[120:123], v231, s[40:43], s71 offen sc1\n" \
  "s_add_u32 s71, s71, 1024\n" \
  "v_mfma_f32_16x16x32_bf16 v[60:63], v[192:195], v[136:139], v[60:63]\n" \
  "v_mfma_f32_16x16x32_bf16 v[64:67], v[196:199], v[124:127], v[64:67]\n" \
  "s_mov_b32 m0, s67\n" \
  "s_add_u32 s67, s67, 4096\n" \
  "buffer_load_dwordx4 v231, s[40:43], s66 offen sc1 lds\n" \
  "s_add_u32 s66, s66, 4096\n" \
  "v_mfma_f32_16x16x32_bf16 v[68:71], v[196:199], v[128:131], v[68:71]\n" \
  "v_mfma_f32_16x16x32_bf16 v[72:75], v[196:199], v[132:135], v[72:75]\n" \
  "s_mov_b32 m0, s67\n" \
  "s_add_u32 s67, s67, 4096\n" \
  "buffer_load_dwordx4 v231, s[40:43], s66 offen sc1 lds\n" \
  "s_add_u32 s66, s66, 4096\n" \
  "s_setprio 2\n" \
  "v_mfma_f32_16x16x32_bf16 v[76:79], v[196:199], v[136:139], v[76:79]\n" \
  "s_setprio 0\n" \
  "s_waitcnt vmcnt(11)\n" \
  "v_mfma_f32_16x16x32_bf16 v[0:3], v[140:143], v[92:95], v[0:3]\n" \
  "buffer_load_dwordx4 v[180:183], v231, s[40:43], s64 offen offset:0 sc1\n" \
  "s_setprio 2\n" \
  "v_mfma_f32_16x16x32_bf16 v[4:7], v[140:143], v[96:99], v[4:7]\n" \
  "s_setprio 0\n" \
  "buffer_load_dwordx4 v[184:187], v231, s[40:43], s64 offen offset:1024 sc1\n" \
  "v_mfma_f32_16x16x32_bf16 v[8:11], v[140:143], v[100:103], v[8:11]\n" \
  "v_mfma_f32_16x16x32_bf16 v[12:15], v[140:143], v[104:107], v[12:15]\n" \
  "buffer_load_dwordx4 v[188:191], v231, s[40:43], s64 offen offset:2048 sc1\n" \
  "v_mfma_f32_16x16x32_bf16 v[16:19], v[144:147], v[92:95], v[16:19]\n" \
  "v_mfma_f32_16x16x32_bf16 v[20:23], v[144:147], v[96:99], v[20:23]\n" \
  "buffer_load_dwordx4 v[192:195], v231, s[40:43], s64 offen offset:3072 sc1\n" \
  "s_add_u32 s64, s64, 4096\n" \
  "v_mfma_f32_16x16x32_bf16 v[24:27], v[144:147], v[100:103], v[24:27]\n" \
  "buffer_load_dwordx4 v[196:199], v231, s[40:43], s64 offen offset:0 sc1\n" \
  "s_add_u32 s64, s64, 1024\n" \
  "s_setprio 2\n" \
  "v_mfma_f32_16x16x32_bf16 v[28:31], v[144:147], v[104:107], v[28:31]\n" \
  "s_setprio 0\n" \
  "v_mfma_f32_16x16x32_bf16 v[32:35], v[148:151], v[92:95], v[32:35]\n" \
  "buffer_load_dwordx4 v[124:127], v231, s[40:43], s68 offen sc1\n" \
  "s_add_u32 s68, s68, 1024\n" \
  "v_mfma_f32_16x16x32_bf16 v[36:39], v[148:151], v[96:99], v[36:39]\n" \
  "v_mfma_f32_16x16x32_bf16 v[40:43], v[148:151], v[100:103], v[40:43]\n" \
  "buffer_load_dwordx4 v[128:131], v231, s[40:43], s69 offen sc1\n" \
  "s_add_u32 s69, s69, 1024\n" \
  "v_mfma_f32_16x16x32_bf16 v[44:47], v[148:151], v[104:107], v[44:47]\n" \
  "buffer_load_dwordx4 v[132:135], v231, s[40:43], s70 offen sc1\n" \
  "s_add_u32 s70, s70, 1024\n" \
  "v_mfma_f32_16x16x32_bf16 v[48:51], v[152:155], v[92:95], v[48:51]\n" \
  "s_setprio 2\n" \
  "v_mfma_f32_16x16x32_bf16 v[52:55], v[152:155], v[96:99], v[52:55]\n" \
  "s_setprio 0\n" \
  "buffer_load_dwordx4 v[136:139], v231, s[40:43], s71 offen sc1\n" \
  "s_add_u32 s71, s71, 1024\n" \
  "v_mfma_f32_16x16x32_bf16 v[56:59], v[152:155], v[100:103], v[56:59]\n" \
  "v_mfma_f32_16x16x32_bf16 v[60:63], v[152:155], v[104:107], v[60:63]\n" \
  "s_mov_b32 m0, s67\n" \
  "s_add_u32 s67, s67, 4096\n" \
  "buffer_load_dwordx4 v231, s[40:43], s66 offen sc1 lds\n" \
  "s_add_u32 s66, s66, 4096\n" \
  "v_mfma_f32_16x16x32_bf16 v[64:67], v[156:159], v[92:95], v[64:67]\n" \
  "s_mov_b32 m0, s67\n" \
  "s_add_u32 s67, s67, 4096\n" \
  "buffer_load_dwordx4 v231, s[40:43], s66 offen sc1 lds\n" \
  "s_add_u32 s66, s66, 4096\n" \
  "v_mfma_f32_16x16x32_bf16 v[68:71], v[156:159], v[96:99], v[68:71]\n" \
  "v_mfma_f32_16x16x32_bf16 v[72:75], v[156:159], v[100:103], v[72:75]\n" \
  "s_mov_b32 m0, s67\n" \
  "s_add_u32 s67, s67, 4096\n" \
  "buffer_load_dwordx4 v231, s[40:43], s66 offen sc1 lds\n" \
  "s_add_u32 s66, s66, 4096\n" \
  "s_setprio 2\n" \
  "v_mfma_f32_16x16x32_bf16 v[76:79], v[156:159], v[104:107], v[76:79]\n" \
  "s_setprio 0\n" \
  "s_sub_u32 s39, s39, 1\n" \
  "s_cbranch_scc0 1b\n" \
  "s_waitcnt vmcnt(12)\n" \
  "v_mfma_f32_16x16x32_bf16 v[0:3], v[160:163], v[108:111], v[0:3]\n" \
  "s_mov_b32 m0, s67\n" \
  "s_add_u32 s67, s67, 4096\n" \
  "buffer_load_dwordx4 v231, s[40:43], s66 offen sc1 lds\n" \
  "s_add_u32 s66, s66, 4096\n" \
  "s_setprio 2\n" \
  "v_mfma_f32_16x16x32_bf16 v[4:7], v[160:163], v[112:115], v[4:7]\n" \
  "s_setprio 0\n" \
  "v_mfma_f32_16x16x32_bf16 v[8:11], v[160:163], v[116:119], v[8:11]\n" \
  "v_mfma_f32_16x16x32_bf16 v[12:15], v[160:163], v[120:123], v[12:15]\n" \
  "s_mov_b32 m0, s67\n" \
  "s_add_u32 s67, s67, 4096\n" \
  "buffer_load_dwordx4 v231, s[40:43], s66 offen sc1 lds\n" \
  "s_add_u32 s66, s66, 4096\n" \
  "v_mfma_f32_16x16x32_bf16 v[16:19], v[164:167], v[108:111], v[16:19]\n" \
  "v_mfma_f32_16x16x32_bf16 v[20:23], v[164:167], v[112:115], v[20:23]\n" \
  "v_mfma_f32_16x16x32_bf16 v[24:27], v[164:167], v[116:119], v[24:27]\n" \
  "buffer_load_dwordx4 v[236:239], v230, s[52:55], s80 offen offset:0\n" \
  "s_setprio 2\n" \
  "v_mfma_f32_16x16x32_bf16 v[28:31], v[164:167], v[120:123], v[28:31]\n" \
  "s_setprio 0\n" \
  "v_mfma_f32_16x16x32_bf16 v[32:35], v[168:171], v[108:111], v[32:35]\n" \
  "v_mfma_f32_16x16x32_bf16 v[36:39], v[168:171], v[112:115], v[36:39]\n" \
  "v_mfma_f32_16x16x32_bf16 v[40:43], v[168:171], v[116:119], v[40:43]\n" \
  "buffer_load_dwordx4 v[240:243], v230, s[52:55], s80 offen offset:16\n" \
  "v_mfma_f32_16x16x32_bf16 v[44:47], v[168:171], v[120:123], v[44:47]\n" \
  "v_mfma_f32_16x16x32_bf16 v[48:51], v[172:175], v[108:111], v[48:51]\n" \
  "s_setprio 2\n" \
  "v_mfma_f32_16x16x32_bf16 v[52:55], v[172:175], v[112:115], v[52:55]\n" \
  "s_setprio 0\n" \
  "buffer_load_dwordx2 v[244:245], v230, s[52:55], s80 offen offset:32\n" \
  "v_mfma_f32_16x16x32_bf16 v[56:59], v[172:175], v[116:119], v[56:59]\n" \
  "v_mfma_f32_16x16x32_bf16 v[60:63], v[172:175], v[120:123], v[60:63]\n" \
  "v_mfma_f32_16x16x32_bf16 v[64:67], v[176:179], v[108:111], v[64:67]\n" \
  "buffer_load_dword v246, v235, s[40:43], s81 offen sc1\n" \
  "v_mfma_f32_16x16x32_bf16 v[68:71], v[176:179], v[112:115], v[68:71]\n" \
  "v_mfma_f32_16x16x32_bf16 v[72:75], v[176:179], v[116:119], v[72:75]\n" \
  "s_setprio 2\n" \
  "v_mfma_f32_16x16x32_bf16 v[76:79], v[176:179], v[120:123], v[76:79]\n" \
  "s_setprio 0\n" \
  "s_waitcnt vmcnt(6)\n" \
  "v_mfma_f32_16x16x32_bf16 v[0:3], v[180:183], v[124:127], v[0:3]\n" \
  "global_load_dwordx4 v[80:83], v234, s[62:63] offset:0\n" \
  "s_setprio 2\n" \
  "v_mfma_f32_16x16x32_bf16 v[4:7], v[180:183], v[128:131], v[4:7]\n" \
  "s_setprio 0\n" \
  "v_mfma_f32_16x16x32_bf16 v[8:11], v[180:183], v[132:135], v[8:11]\n" \
  "v_mfma_f32_16x16x32_bf16 v[12:15], v[180:183], v[136:139], v[12:15]\n" \
  "v_mfma_f32_16x16x32_bf16 v[16:19], v[184:187], v[124:127], v[16:19]\n" \
  "v_mfma_f32_16x16x32_bf16 v[20:23], v[184:187], v[128:131], v[20:23]\n" \
  "global_load_dwordx4 v[84:87], v234, s[62:63] offset:16\n" \
  "v_mfma_f32_16x16x32_bf16 v[24:27], v[184:187], v[132:135], v[24:27]\n" \
  "s_setprio 2\n" \
  "v_mfma_f32_16x16x32_bf16 v[28:31], v[184:187], v[136:139], v[28:31]\n" \
  "s_setprio 0\n" \
  "v_mfma_f32_16x16x32_bf16 v[32:35], v[188:191], v[124:127], v[32:35]\n" \
  "v_mfma_f32_16x16x32_bf16 v[36:39], v[188:191], v[128:131], v[36:39]\n" \
  "v_mfma_f32_16x16x32_bf16 v[40:43], v[188:191], v[132:135], v[40:43]\n" \
  "global_load_dwordx2 v[88:89], v234, s[62:63] offset:32\n" \
  "v_mfma_f32_16x16x32_bf16 v[44:47], v[188:191], v[136:139], v[44:47]\n" \
  "v_mfma_f32_16x16x32_bf16 v[48:51], v[192:195], v[124:127], v[48:51]\n" \
  "s_setprio 2\n" \
  "v_mfma_f32_16x16x32_bf16 v[52:55], v[192:195], v[128:131], v[52:55]\n" \
  "s_setprio 0\n" \
  "v_mfma_f32_16x16x32_bf16 v[56:59], v[192:195], v[132:135], v[56:59]\n" \
  "v_mfma_f32_16x16x32_bf16 v[60:63], v[192:195], v[136:139], v[60:63]\n" \
  "buffer_load_dword v90, v233, s[40:43], s77 offen sc1\n" \
  "v_mfma_f32_16x16x32_bf16 v[64:67], v[196:199], v[124:127], v[64:67]\n" \
  "v_mfma_f32_16x16x32_bf16 v[68:71], v[196:199], v[128:131], v[68:71]\n" \
  "v_mfma_f32_16x16x32_bf16 v[72:75], v[196:199], v[132:135], v[72:75]\n" \
  "s_setprio 2\n" \
  "v_mfma_f32_16x16x32_bf16 v[76:79], v[196:199], v[136:139], v[76:79]\n" \
  "s_setprio 0\n" \
    \
  "s_setprio 2\n" \
  "s_nop 3\n" \
  "v_add_u32 v249, s73, v231\n" \
  "ds_write_b128 v249, v[4:7] offset:0\n" \
  "ds_write_b128 v249, v[20:23] offset:1024\n" \
  "ds_write_b128 v249, v[36:39] offset:2048\n" \
  "ds_write_b128 v249, v[52:55] offset:3072\n" \
  "ds_write_b128 v249, v[68:71] offset:4096\n" \
  "v_add_u32 v249, s74, v231\n" \
  "ds_write_b128 v249, v[8:11] offset:5120\n" \
  "ds_write_b128 v249, v[24:27] offset:6144\n" \
  "ds_write_b128 v249, v[40:43] offset:7168\n" \
  "ds_write_b128 v249, v[56:59] offset:8192\n" \
  "ds_write_b128 v249, v[72:75] offset:9216\n" \
  "v_add_u32 v249, s75, v231\n" \
  "ds_write_b128 v249, v[12:15] offset:10240\n" \
  "ds_write_b128 v249, v[28:31] offset:11264\n" \
  "ds_write_b128 v249, v[44:47] offset:12288\n" \
  "ds_write_b128 v249, v[60:63] offset:13312\n" \
  "ds_write_b128 v249, v[76:79] offset:14336\n" \
  "v_mov_b32 v250, 5\n" \
  "s_waitcnt lgkmcnt(0)\n" \
  "ds_write_b32 v248, v250\n" \
  "s_mov_b32 s83, 1048576\n" \
  "5:\n" \
  "ds_read_b128 v[250:253], v247\n" \
  "s_waitcnt lgkmcnt(0)\n" \
  "v_min3_u32 v250, v250, v251, v252\n" \
  "v_min_u32 v250, v250, v253\n" \
  "v_cmp_gt_u32 vcc, 5, v250\n" \
  "s_cbranch_vccz 6f\n" \
  "s_sleep 2\n" \
  "s_sub_u32 s83, s83, 1\n" \
  "s_cbranch_scc0 5b\n" \
  "6:\n" \
  "v_add_u32 v249, s72, v231\n" \
  "ds_read_b128 v[4:7], v249 offset:0\n" \
  "ds_read_b128 v[20:23], v249 offset:1024\n" \
  "ds_read_b128 v[36:39], v249 offset:2048\n" \
  "ds_read_b128 v[52:55], v249 offset:3072\n" \
  "ds_read_b128 v[68:71], v249 offset:4096\n" \
  "ds_read_b128 v[8:11], v249 offset:5120\n" \
  "ds_read_b128 v[24:27], v249 offset:6144\n" \
  "ds_read_b128 v[40:43], v249 offset:7168\n" \
  "ds_read_b128 v[56:59], v249 offset:8192\n" \
  "ds_read_b128 v[72:75], v249 offset:9216\n" \
  "ds_read_b128 v[12:15], v249 offset:10240\n" \
  "ds_read_b128 v[28:31], v249 offset:11264\n" \
  "ds_read_b128 v[44:47], v249 offset:12288\n" \
  "ds_read_b128 v[60:63], v249 offset:13312\n" \
  "ds_read_b128 v[76:79], v249 offset:14336\n" \
  "v_mov_b32 v250, 6\n" \
  "s_waitcnt lgkmcnt(0)\n" \
  "ds_write_b32 v248, v250\n" \
  "s_mov_b32 s83, 1048576\n" \
  "5:\n" \
  "ds_read_b128 v[250:253], v247\n" \
  "s_waitcnt lgkmcnt(0)\n" \
  "v_min3_u32 v250, v250, v251, v252\n" \
  "v_min_u32 v250, v250, v253\n" \
  "v_cmp_gt_u32 vcc, 6, v250\n" \
  "s_cbranch_vccz 6f\n" \
  "s_sleep 2\n" \
  "s_sub_u32 s83, s83, 1\n" \
  "s_cbranch_scc0 5b\n" \
  "6:\n" \
  "s_mov_b32 m0, s67\n" \
  "s_add_u32 s67, s67, 4096\n" \
  "buffer_load_dwordx4 v231, s[40:43], s66 offen sc1 lds\n" \
  "s_add_u32 s66, s66, 4096\n" \
  "s_mov_b32 m0, s67\n" \
  "s_add_u32 s67, s67, 4096\n" \
  "buffer_load_dwordx4 v231, s[40:43], s66 offen sc1 lds\n" \
  "s_add_u32 s66, s66, 4096\n" \
  "v_add_u32 v249, 51200, v231\n" \
  "ds_read_b128 v[100:103], v231 offset:0\n" \
  "ds_read_b128 v[104:107], v231 offset:1024\n" \
  "ds_read_b128 v[108:111], v231 offset:2048\n" \
  "ds_read_b128 v[112:115], v231 offset:3072\n" \
  "ds_read_b128 v[116:119], v231 offset:4096\n" \
  "ds_read_b128 v[120:123], v231 offset:5120\n" \
  "ds_read_b128 v[124:127], v231 offset:6144\n" \
  "ds_read_b128 v[128:131], v231 offset:7168\n" \
  "ds_read_b128 v[132:135], v231 offset:8192\n" \
  "ds_read_b128 v[136:139], v231 offset:9216\n" \
  "s_waitcnt lgkmcnt(0)\n" \
  "ds_read_b128 v[140:143], v231 offset:10240\n" \
  "ds_read_b128 v[144:147], v231 offset:11264\n" \
  "ds_read_b128 v[148:151], v231 offset:12288\n" \
  "ds_read_b128 v[152:155], v231 offset:13312\n" \
  "ds_read_b128 v[156:159], v231 offset:14336\n" \
  "ds_read_b128 v[160:163], v231 offset:15360\n" \
  "ds_read_b128 v[164:167], v231 offset:16384\n" \
  "ds_read_b128 v[168:171], v231 offset:17408\n" \
  "ds_read_b128 v[172:175], v231 offset:18432\n" \
  "ds_read_b128 v[176:179], v231 offset:19456\n" \
  "s_waitcnt lgkmcnt(0)\n" \
  "ds_read_b128 v[180:183], v231 offset:20480\n" \
  "ds_read_b128 v[184:187], v231 offset:21504\n" \
  "ds_read_b128 v[188:191], v231 offset:22528\n" \
  "ds_read_b128 v[192:195], v231 offset:23552\n" \
  "ds_read_b128 v[196:199], v231 offset:24576\n" \
  "s_waitcnt vmcnt(0)\n" \
    \
  "s_setprio 0\n" \
  "s_barrier\n" \
  "s_barrier\n" \
  "v_pk_add_f32 v[0:1], v[0:1], v[4:5]\n" \
  "v_pk_add_f32 v[2:3], v[2:3], v[6:7]\n" \
  "v_pk_add_f32 v[16:17], v[16:17], v[20:21]\n" \
  "v_pk_add_f32 v[18:19], v[18:19], v[22:23]\n" \
  "v_pk_add_f32 v[32:33], v[32:33], v[36:37]\n" \
  "v_pk_add_f32 v[34:35], v[34:35], v[38:39]\n" \
  "v_pk_add_f32 v[48:49], v[48:49], v[52:53]\n" \
  "v_pk_add_f32 v[50:51], v[50:51], v[54:55]\n" \
  "v_pk_add_f32 v[64:65], v[64:65], v[68:69]\n" \
  "v_pk_add_f32 v[66:67], v[66:67], v[70:71]\n" \
  "v_pk_add_f32 v[0:1], v[0:1], v[8:9]\n" \
  "v_pk_add_f32 v[2:3], v[2:3], v[10:11]\n" \
  "v_pk_add_f32 v[16:17], v[16:17], v[24:25]\n" \
  "v_pk_add_f32 v[18:19], v[18:19], v[26:27]\n" \
  "v_pk_add_f32 v[32:33], v[32:33], v[40:41]\n" \
  "v_pk_add_f32 v[34:35], v[34:35], v[42:43]\n" \
  "v_pk_add_f32 v[48:49], v[48:49], v[56:57]\n" \
  "v_pk_add_f32 v[50:51], v[50:51], v[58:59]\n" \
  "v_pk_add_f32 v[64:65], v[64:65], v[72:73]\n" \
  "v_pk_add_f32 v[66:67], v[66:67], v[74:75]\n" \
  "v_pk_add_f32 v[0:1], v[0:1], v[12:13]\n" \
  "v_pk_add_f32 v[2:3], v[2:3], v[14:15]\n" \
  "v_pk_add_f32 v[16:17], v[16:17], v[28:29]\n" \
  "v_pk_add_f32 v[18:19], v[18:19], v[30:31]\n" \
  "v_pk_add_f32 v[32:33], v[32:33], v[44:45]\n" \
  "v_pk_add_f32 v[34:35], v[34:35], v[46:47]\n" \
  "v_pk_add_f32 v[48:49], v[48:49], v[60:61]\n" \
  "v_pk_add_f32 v[50:51], v[50:51], v[62:63]\n" \
  "v_pk_add_f32 v[64:65], v[64:65], v[76:77]\n" \
  "v_pk_add_f32 v[66:67], v[66:67], v[78:79]\n" \
  "v_mul_f32 v246, v246, v246\n" \
  "v_lshlrev_b32 v200, 16, v80\n" \
  "v_and_b32 v201, 0xffff0000, v80\n" \
  "v_lshlrev_b32 v202, 16, v81\n" \
  "v_and_b32 v203, 0xffff0000, v81\n" \
  "v_lshlrev_b32 v204, 16, v82\n" \
  "v_and_b32 v205, 0xffff0000, v82\n" \
  "v_lshlrev_b32 v206, 16, v83\n" \
  "v_and_b32 v207, 0xffff0000, v83\n" \
  "v_lshlrev_b32 v208, 16, v84\n" \
  "v_and_b32 v209, 0xffff0000, v84\n" \
  "v_lshlrev_b32 v210, 16, v85\n" \
  "v_and_b32 v211, 0xffff0000, v85\n" \
  "v_lshlrev_b32 v212, 16, v86\n" \
  "v_and_b32 v213, 0xffff0000, v86\n" \
  "v_lshlrev_b32 v214, 16, v87\n" \
  "v_and_b32 v215, 0xffff0000, v87\n" \
  "v_lshlrev_b32 v216, 16, v88\n" \
  "v_and_b32 v217, 0xffff0000, v88\n" \
  "v_lshlrev_b32 v218, 16, v89\n" \
  "v_and_b32 v219, 0xffff0000, v89\n" \
  "v_fma_f32 v0, v0, v246, v200\n" \
  "v_fma_f32 v1, v1, v246, v201\n" \
  "v_fma_f32 v2, v2, v246, v202\n" \
  "v_fma_f32 v3, v3, v246, v203\n" \
  "v_fma_f32 v16, v16, v246, v204\n" \
  "v_fma_f32 v17, v17, v246, v205\n" \
  "v_fma_f32 v18, v18, v246, v206\n" \
  "v_fma_f32 v19, v19, v246, v207\n" \
  "v_fma_f32 v32, v32, v246, v208\n" \
  "v_fma_f32 v33, v33, v246, v209\n" \
  "v_fma_f32 v34, v34, v246, v210\n" \
  "v_fma_f32 v35, v35, v246, v211\n" \
  "v_fma_f32 v48, v48, v246, v212\n" \
  "v_fma_f32 v49, v49, v246, v213\n" \
  "v_fma_f32 v50, v50, v246, v214\n" \
  "v_fma_f32 v51, v51, v246, v215\n" \
  "v_fma_f32 v64, v64, v246, v216\n" \
  "v_fma_f32 v65, v65, v246, v217\n" \
  "v_fma_f32 v66, v66, v246, v218\n" \
  "v_fma_f32 v67, v67, v246, v219\n" \
  "v_mul_f32 v200, 0xbfb8aa3b, v0\n" \
  "v_mul_f32 v201, 0xbfb8aa3b, v1\n" \
  "v_mul_f32 v202, 0xbfb8aa3b, v2\n" \
  "v_mul_f32 v203, 0xbfb8aa3b, v3\n" \
  "v_mul_f32 v204, 0xbfb8aa3b, v16\n" \
  "v_mul_f32 v205, 0xbfb8aa3b, v17\n" \
  "v_mul_f32 v206, 0xbfb8aa3b, v18\n" \
  "v_mul_f32 v207, 0xbfb8aa3b, v19\n" \
  "v_mul_f32 v208, 0xbfb8aa3b, v32\n" \
  "v_mul_f32 v209, 0xbfb8aa3b, v33\n" \
  "v_mul_f32 v210, 0xbfb8aa3b, v34\n" \
  "v_mul_f32 v211, 0xbfb8aa3b, v35\n" \
  "v_mul_f32 v212, 0xbfb8aa3b, v48\n" \
  "v_mul_f32 v213, 0xbfb8aa3b, v49\n" \
  "v_mul_f32 v214, 0xbfb8aa3b, v50\n" \
  "v_mul_f32 v215, 0xbfb8aa3b, v51\n" \
  "v_mul_f32 v216, 0xbfb8aa3b, v64\n" \
  "v_mul_f32 v217, 0xbfb8aa3b, v65\n" \
  "v_mul_f32 v218, 0xbfb8aa3b, v66\n" \
  "v_mul_f32 v219, 0xbfb8aa3b, v67\n" \
  "v_exp_f32 v200, v200\n" \
  "v_exp_f32 v201, v201\n" \
  "v_exp_f32 v202, v202\n" \
  "v_exp_f32 v203, v203\n" \
  "v_exp_f32 v204, v204\n" \
  "v_exp_f32 v205, v205\n" \
  "v_exp_f32 v206, v206\n" \
  "v_exp_f32 v207, v207\n" \
  "v_exp_f32 v208, v208\n" \
  "v_exp_f32 v209, v209\n" \
  "v_exp_f32 v210, v210\n" \
  "v_exp_f32 v211, v211\n" \
  "v_exp_f32 v212, v212\n" \
  "v_exp_f32 v213, v213\n" \
  "v_exp_f32 v214, v214\n" \
  "v_exp_f32 v215, v215\n" \
  "v_exp_f32 v216, v216\n" \
  "v_exp_f32 v217, v217\n" \
  "v_exp_f32 v218, v218\n" \
  "v_exp_f32 v219, v219\n" \
  "v_add_f32 v200, 1.0, v200\n" \
  "v_add_f32 v201, 1.0, v201\n" \
  "v_add_f32 v202, 1.0, v202\n" \
  "v_add_f32 v203, 1.0, v203\n" \
  "v_add_f32 v204, 1.0, v204\n" \
  "v_add_f32 v205, 1.0, v205\n" \
  "v_add_f32 v206, 1.0, v206\n" \
  "v_add_f32 v207, 1.0, v207\n" \
  "v_add_f32 v208, 1.0, v208\n" \
  "v_add_f32 v209, 1.0, v209\n" \
  "v_add_f32 v210, 1.0, v210\n" \
  "v_add_f32 v211, 1.0, v211\n" \
  "v_add_f32 v212, 1.0, v212\n" \
  "v_add_f32 v213, 1.0, v213\n" \
  "v_add_f32 v214, 1.0, v214\n" \
  "v_add_f32 v215, 1.0, v215\n" \
  "v_add_f32 v216, 1.0, v216\n" \
  "v_add_f32 v217, 1.0, v217\n" \
  "v_add_f32 v218, 1.0, v218\n" \
  "v_add_f32 v219, 1.0, v219\n" \
  "v_rcp_f32 v200, v200\n" \
  "v_rcp_f32 v201, v201\n" \
  "v_rcp_f32 v202, v202\n" \
  "v_rcp_f32 v203, v203\n" \
  "v_rcp_f32 v204, v204\n" \
  "v_rcp_f32 v205, v205\n" \
  "v_rcp_f32 v206, v206\n" \
  "v_rcp_f32 v207, v207\n" \
  "v_rcp_f32 v208, v208\n" \
  "v_rcp_f32 v209, v209\n" \
  "v_rcp_f32 v210, v210\n" \
  "v_rcp_f32 v211, v211\n" \
  "v_rcp_f32 v212, v212\n" \
  "v_rcp_f32 v213, v213\n" \
  "v_rcp_f32 v214, v214\n" \
  "v_rcp_f32 v215, v215\n" \
  "v_rcp_f32 v216, v216\n" \
  "v_rcp_f32 v217, v217\n" \
  "v_rcp_f32 v218, v218\n" \
  "v_rcp_f32 v219, v219\n" \
  "v_pk_mul_f32 v[0:1], v[0:1], v[200:201]\n" \
  "v_pk_mul_f32 v[2:3], v[2:3], v[202:203]\n" \
  "v_pk_mul_f32 v[16:17], v[16:17], v[204:205]\n" \
  "v_pk_mul_f32 v[18:19], v[18:19], v[206:207]\n" \
  "v_pk_mul_f32 v[32:33], v[32:33], v[208:209]\n" \
  "v_pk_mul_f32 v[34:35], v[34:35], v[210:211]\n" \
  "v_pk_mul_f32 v[48:49], v[48:49], v[212:213]\n" \
  "v_pk_mul_f32 v[50:51], v[50:51], v[214:215]\n" \
  "v_pk_mul_f32 v[64:65], v[64:65], v[216:217]\n" \
  "v_pk_mul_f32 v[66:67], v[66:67], v[218:219]\n" \
  "s_mul_i32 s34, s29, 3072\n" \
  "s_add_u32 s34, s34, 102400\n" \
  "v_mul_u32_u24 v226, 10, v235\n" \
  "v_add3_u32 v226, v226, v234, s34\n" \
  "v_add_u32 v227, s34, v231\n" \
  "s_add_u32 s34, s6, 21504\n" \
  "s_addc_u32 s35, s7, 0\n" \
  "global_load_dwordx4 v[220:223], v231, s[34:35]\n" \
    \
  "s_sleep 16\n" \
  "s_mov_b32 s83, 1048576\n" \
  "7:\n" \
  "buffer_load_dwordx2 v[250:251], v233, s[40:43], s77 offen sc1\n" \
  "s_waitcnt vmcnt(0)\n" \
  "v_cmp_ne_u64 vcc, s[60:61], v[250:251]\n" \
  "s_cbranch_vccz 8f\n" \
  "s_mov_b64 exec, vcc\n" \
  "s_sleep 1\n" \
  "s_sub_u32 s83, s83, 1\n" \
  "s_cbranch_scc0 7b\n" \
  "8:\n" \
  "s_mov_b64 exec, -1\n" \
    \
  "buffer_load_dwordx4 v[4:7], v228, s[40:43], s78 offen sc1\n" \
  "s_add_u32 s78, s78, 5120\n" \
  "buffer_load_dwordx4 v[8:11], v228, s[40:43], s78 offen sc1\n" \
  "s_add_u32 s78, s78, 5120\n" \
  "buffer_load_dwordx4 v[12:15], v228, s[40:43], s78 offen sc1\n" \
  "s_add_u32 s78, s78, 5120\n" \
  "buffer_load_dwordx4 v[20:23], v228, s[40:43], s78 offen sc1\n" \
  "s_add_u32 s78, s78, 5120\n" \
  "buffer_load_dwordx4 v[24:27], v228, s[40:43], s78 offen sc1\n" \
  "s_add_u32 s78, s78, 5120\n" \
  "buffer_load_dwordx4 v[28:31], v228, s[40:43], s78 offen sc1\n" \
  "s_add_u32 s78, s78, 5120\n" \
  "buffer_load_dwordx4 v[36:39], v228, s[40:43], s78 offen sc1\n" \
  "s_add_u32 s78, s78, 5120\n" \
  "buffer_load_dwordx4 v[40:43], v228, s[40:43], s78 offen sc1\n" \
  "s_add_u32 s78, s78, 5120\n" \
  "buffer_load_dwordx4 v[44:47], v228, s[40:43], s78 offen sc1\n" \
  "s_add_u32 s78, s78, 5120\n" \
  "buffer_load_dwordx4 v[52:55], v228, s[40:43], s78 offen sc1\n" \
  "s_add_u32 s78, s78, 5120\n" \
  "s_waitcnt vmcnt(9) lgkmcnt(0)\n" \
  "v_mfma_f32_16x16x32_bf16 v[200:203], v[100:103], v[4:7], 0\n" \
  "buffer_load_dwordx4 v[56:59], v228, s[40:43], s78 offen sc1\n" \
  "s_add_u32 s78, s78, 5120\n" \
  "v_mfma_f32_16x16x32_bf16 v[204:207], v[104:107], v[4:7], 0\n" \
  "v_mfma_f32_16x16x32_bf16 v[208:211], v[108:111], v[4:7], 0\n" \
  "v_mfma_f32_16x16x32_bf16 v[212:215], v[112:115], v[4:7], 0\n" \
  "v_mfma_f32_16x16x32_bf16 v[216:219], v[116:119], v[4:7], 0\n" \
  "s_waitcnt vmcnt(9)\n" \
  "v_mfma_f32_16x16x32_bf16 v[200:203], v[120:123], v[8:11], v[200:203]\n" \
  "buffer_load_dwordx4 v[60:63], v228, s[40:43], s78 offen sc1\n" \
  "s_add_u32 s78, s78, 5120\n" \
  "ds_read_b128 v[100:103], v231 offset:25600\n" \
  "v_mfma_f32_16x16x32_bf16 v[204:207], v[124:127], v[8:11], v[204:207]\n" \
  "ds_read_b128 v[104:107], v231 offset:26624\n" \
  "v_mfma_f32_16x16x32_bf16 v[208:211], v[128:131], v[8:11], v[208:211]\n" \
  "ds_read_b128 v[108:111], v231 offset:27648\n" \
  "v_mfma_f32_16x16x32_bf16 v[212:215], v[132:135], v[8:11], v[212:215]\n" \
  "ds_read_b128 v[112:115], v231 offset:28672\n" \
  "v_mfma_f32_16x16x32_bf16 v[216:219], v[136:139], v[8:11], v[216:219]\n" \
  "ds_read_b128 v[116:119], v231 offset:29696\n" \
  "s_waitcnt vmcnt(9)\n" \
  "v_mfma_f32_16x16x32_bf16 v[200:203], v[140:143], v[12:15], v[200:203]\n" \
  "buffer_load_dwordx4 v[68:71], v228, s[40:43], s78 offen sc1\n" \
  "s_add_u32 s78, s78, 5120\n" \
  "ds_read_b128 v[120:123], v231 offset:30720\n" \
  "v_mfma_f32_16x16x32_bf16 v[204:207], v[144:147], v[12:15], v[204:207]\n" \
  "ds_read_b128 v[124:127], v231 offset:31744\n" \
  "v_mfma_f32_16x16x32_bf16 v[208:211], v[148:151], v[12:15], v[208:211]\n" \
  "ds_read_b128 v[128:131], v231 offset:32768\n" \
  "v_mfma_f32_16x16x32_bf16 v[212:215], v[152:155], v[12:15], v[212:215]\n" \
  "ds_read_b128 v[132:135], v231 offset:33792\n" \
  "v_mfma_f32_16x16x32_bf16 v[216:219], v[156:159], v[12:15], v[216:219]\n" \
  "ds_read_b128 v[136:139], v231 offset:34816\n" \
  "s_waitcnt vmcnt(9)\n" \
  "v_mfma_f32_16x16x32_bf16 v[200:203], v[160:163], v[20:23], v[200:203]\n" \
  "buffer_load_dwordx4 v[72:75], v228, s[40:43], s78 offen sc1\n" \
  "s_add_u32 s78, s78, 5120\n" \
  "ds_read_b128 v[140:143], v231 offset:35840\n" \
  "v_mfma_f32_16x16x32_bf16 v[204:207], v[164:167], v[20:23], v[204:207]\n" \
  "ds_read_b128 v[144:147], v231 offset:36864\n" \
  "v_mfma_f32_16x16x32_bf16 v[208:211], v[168:171], v[20:23], v[208:211]\n" \
  "ds_read_b128 v[148:151], v231 offset:37888\n" \
  "v_mfma_f32_16x16x32_bf16 v[212:215], v[172:175], v[20:23], v[212:215]\n" \
  "ds_read_b128 v[152:155], v231 offset:38912\n" \
  "v_mfma_f32_16x16x32_bf16 v[216:219], v[176:179], v[20:23], v[216:219]\n" \
  "ds_read_b128 v[156:159], v231 offset:39936\n" \
  "s_waitcnt vmcnt(9)\n" \
  "v_mfma_f32_16x16x32_bf16 v[200:203], v[180:183], v[24:27], v[200:203]\n" \
  "buffer_load_dwordx4 v[76:79], v228, s[40:43], s78 offen sc1\n" \
  "s_add_u32 s78, s78, 5120\n" \
  "ds_read_b128 v[160:163], v231 offset:40960\n" \
  "v_mfma_f32_16x16x32_bf16 v[204:207], v[184:187], v[24:27], v[204:207]\n" \
  "ds_read_b128 v[164:167], v231 offset:41984\n" \
  "v_mfma_f32_16x16x32_bf16 v[208:211], v[188:191], v[24:27], v[208:211]\n" \
  "ds_read_b128 v[168:171], v231 offset:43008\n" \
  "v_mfma_f32_16x16x32_bf16 v[212:215], v[192:195], v[24:27], v[212:215]\n" \
  "ds_read_b128 v[172:175], v231 offset:44032\n" \
  "v_mfma_f32_16x16x32_bf16 v[216:219], v[196:199], v[24:27], v[216:219]\n" \
  "ds_read_b128 v[176:179], v231 offset:45056\n" \
  "s_waitcnt vmcnt(9) lgkmcnt(10)\n" \
  "v_mfma_f32_16x16x32_bf16 v[200:203], v[100:103], v[28:31], v[200:203]\n" \
  "buffer_load_dwordx4 v[80:83], v228, s[40:43], s78 offen sc1\n" \
  "s_add_u32 s78, s78, 5120\n" \
  "ds_read_b128 v[180:183], v231 offset:46080\n" \
  "v_mfma_f32_16x16x32_bf16 v[204:207], v[104:107], v[28:31], v[204:207]\n" \
  "ds_read_b128 v[184:187], v231 offset:47104\n" \
  "v_mfma_f32_16x16x32_bf16 v[208:211], v[108:111], v[28:31], v[208:211]\n" \
  "ds_read_b128 v[188:191], v231 offset:48128\n" \
  "v_mfma_f32_16x16x32_bf16 v[212:215], v[112:115], v[28:31], v[212:215]\n" \
  "ds_read_b128 v[192:195], v231 offset:49152\n" \
  "v_mfma_f32_16x16x32_bf16 v[216:219], v[116:119], v[28:31], v[216:219]\n" \
  "ds_read_b128 v[196:199], v231 offset:50176\n" \
  "s_waitcnt vmcnt(9) lgkmcnt(10)\n" \
  "v_mfma_f32_16x16x32_bf16 v[200:203], v[120:123], v[36:39], v[200:203]\n" \
  "buffer_load_dwordx2 v[84:85], v229, s[40:43], s79 offen sc1\n" \
  "s_add_u32 s79, s79, 10240\n" \
  "ds_read_b128 v[100:103], v249 offset:0\n" \
  "v_mfma_f32_16x16x32_bf16 v[204:207], v[124:127], v[36:39], v[204:207]\n" \
  "ds_read_b128 v[104:107], v249 offset:1024\n" \
  "v_mfma_f32_16x16x32_bf16 v[208:211], v[128:131], v[36:39], v[208:211]\n" \
  "ds_read_b128 v[108:111], v249 offset:2048\n" \
  "v_mfma_f32_16x16x32_bf16 v[212:215], v[132:135], v[36:39], v[212:215]\n" \
  "ds_read_b128 v[112:115], v249 offset:3072\n" \
  "v_mfma_f32_16x16x32_bf16 v[216:219], v[136:139], v[36:39], v[216:219]\n" \
  "ds_read_b128 v[116:119], v249 offset:4096\n" \
  "s_waitcnt vmcnt(9) lgkmcnt(10)\n" \
  "v_mfma_f32_16x16x32_bf16 v[200:203], v[140:143], v[40:43], v[200:203]\n" \
  "buffer_load_dwordx2 v[86:87], v229, s[40:43], s79 offen sc1\n" \
  "s_add_u32 s79, s79, 10240\n" \
  "ds_read_b128 v[120:123], v249 offset:5120\n" \
  "v_mfma_f32_16x16x32_bf16 v[204:207], v[144:147], v[40:43], v[204:207]\n" \
  "ds_read_b128 v[124:127], v249 offset:6144\n" \
  "v_mfma_f32_16x16x32_bf16 v[208:211], v[148:151], v[40:43], v[208:211]\n" \
  "ds_read_b128 v[128:131], v249 offset:7168\n" \
  "v_mfma_f32_16x16x32_bf16 v[212:215], v[152:155], v[40:43], v[212:215]\n" \
  "ds_read_b128 v[132:135], v249 offset:8192\n" \
  "v_mfma_f32_16x16x32_bf16 v[216:219], v[156:159], v[40:43], v[216:219]\n" \
  "ds_read_b128 v[136:139], v249 offset:9216\n" \
  "s_waitcnt vmcnt(9) lgkmcnt(10)\n" \
  "v_mfma_f32_16x16x32_bf16 v[200:203], v[160:163], v[44:47], v[200:203]\n" \
  "buffer_load_dwordx2 v[88:89], v229, s[40:43], s79 offen sc1\n" \
  "s_add_u32 s79, s79, 10240\n" \
  "ds_read_b128 v[140:143], v249 offset:10240\n" \
  "v_mfma_f32_16x16x32_bf16 v[204:207], v[164:167], v[44:47], v[204:207]\n" \
  "ds_read_b128 v[144:147], v249 offset:11264\n" \
  "v_mfma_f32_16x16x32_bf16 v[208:211], v[168:171], v[44:47], v[208:211]\n" \
  "ds_read_b128 v[148:151], v249 offset:12288\n" \
  "v_mfma_f32_16x16x32_bf16 v[212:215], v[172:175], v[44:47], v[212:215]\n" \
  "ds_read_b128 v[152:155], v249 offset:13312\n" \
  "v_mfma_f32_16x16x32_bf16 v[216:219], v[176:179], v[44:47], v[216:219]\n" \
  "ds_read_b128 v[156:159], v249 offset:14336\n" \
  "s_waitcnt vmcnt(9) lgkmcnt(10)\n" \
  "v_mfma_f32_16x16x32_bf16 v[200:203], v[180:183], v[52:55], v[200:203]\n" \
  "buffer_load_dwordx2 v[90:91], v229, s[40:43], s79 offen sc1\n" \
  "s_add_u32 s79, s79, 10240\n" \
  "ds_read_b128 v[160:163], v249 offset:15360\n" \
  "v_mfma_f32_16x16x32_bf16 v[204:207], v[184:187], v[52:55], v[204:207]\n" \
  "ds_read_b128 v[164:167], v249 offset:16384\n" \
  "v_mfma_f32_16x16x32_bf16 v[208:211], v[188:191], v[52:55], v[208:211]\n" \
  "ds_read_b128 v[168:171], v249 offset:17408\n" \
  "v_mfma_f32_16x16x32_bf16 v[212:215], v[192:195], v[52:55], v[212:215]\n" \
  "ds_read_b128 v[172:175], v249 offset:18432\n" \
  "v_mfma_f32_16x16x32_bf16 v[216:219], v[196:199], v[52:55], v[216:219]\n" \
  "ds_read_b128 v[176:179], v249 offset:19456\n" \
  "s_waitcnt vmcnt(9) lgkmcnt(10)\n" \
  "v_mfma_f32_16x16x32_bf16 v[200:203], v[100:103], v[56:59], v[200:203]\n" \
  "buffer_load_dwordx2 v[92:93], v229, s[40:43], s79 offen sc1\n" \
  "s_add_u32 s79, s79, 10240\n" \
  "ds_read_b128 v[180:183], v249 offset:20480\n" \
  "v_mfma_f32_16x16x32_bf16 v[204:207], v[104:107], v[56:59], v[204:207]\n" \
  "ds_read_b128 v[184:187], v249 offset:21504\n" \
  "v_mfma_f32_16x16x32_bf16 v[208:211], v[108:111], v[56:59], v[208:211]\n" \
  "ds_read_b128 v[188:191], v249 offset:22528\n" \
  "v_mfma_f32_16x16x32_bf16 v[212:215], v[112:115], v[56:59], v[212:215]\n" \
  "ds_read_b128 v[192:195], v249 offset:23552\n" \
  "v_mfma_f32_16x16x32_bf16 v[216:219], v[116:119], v[56:59], v[216:219]\n" \
  "ds_read_b128 v[196:199], v249 offset:24576\n" \
  "s_waitcnt vmcnt(9) lgkmcnt(10)\n" \
  "v_mfma_f32_16x16x32_bf16 v[200:203], v[120:123], v[60:63], v[200:203]\n" \
  "buffer_load_dwordx2 v[94:95], v229, s[40:43], s79 offen sc1\n" \
  "s_add_u32 s79, s79, 10240\n" \
  "ds_read_b128 v[100:103], v249 offset:25600\n" \
  "v_mfma_f32_16x16x32_bf16 v[204:207], v[124:127], v[60:63], v[204:207]\n" \
  "ds_read_b128 v[104:107], v249 offset:26624\n" \
  "v_mfma_f32_16x16x32_bf16 v[208:211], v[128:131], v[60:63], v[208:211]\n" \
  "ds_read_b128 v[108:111], v249 offset:27648\n" \
  "v_mfma_f32_16x16x32_bf16 v[212:215], v[132:135], v[60:63], v[212:215]\n" \
  "ds_read_b128 v[112:115], v249 offset:28672\n" \
  "v_mfma_f32_16x16x32_bf16 v[216:219], v[136:139], v[60:63], v[216:219]\n" \
  "ds_read_b128 v[116:119], v249 offset:29696\n" \
  "s_waitcnt vmcnt(9) lgkmcnt(10)\n" \
  "v_mfma_f32_16x16x32_bf16 v[200:203], v[140:143], v[68:71], v[200:203]\n" \
  "buffer_load_dwordx2 v[96:97], v229, s[40:43], s79 offen sc1\n" \
  "s_add_u32 s79, s79, 10240\n" \
  "ds_read_b128 v[120:123], v249 offset:30720\n" \
  "v_mfma_f32_16x16x32_bf16 v[204:207], v[144:147], v[68:71], v[204:207]\n" \
  "ds_read_b128 v[124:127], v249 offset:31744\n" \
  "v_mfma_f32_16x16x32_bf16 v[208:211], v[148:151], v[68:71], v[208:211]\n" \
  "ds_read_b128 v[128:131], v249 offset:32768\n" \
  "v_mfma_f32_16x16x32_bf16 v[212:215], v[152:155], v[68:71], v[212:215]\n" \
  "ds_read_b128 v[132:135], v249 offset:33792\n" \
  "v_mfma_f32_16x16x32_bf16 v[216:219], v[156:159], v[68:71], v[216:219]\n" \
  "ds_read_b128 v[136:139], v249 offset:34816\n" \
  "s_waitcnt vmcnt(9) lgkmcnt(10)\n" \
  "v_mfma_f32_16x16x32_bf16 v[200:203], v[160:163], v[72:75], v[200:203]\n" \
  "buffer_load_dwordx2 v[98:99], v229, s[40:43], s79 offen sc1\n" \
  "s_add_u32 s79, s79, 10240\n" \
  "ds_read_b128 v[140:143], v249 offset:35840\n" \
  "v_mfma_f32_16x16x32_bf16 v[204:207], v[164:167], v[72:75], v[204:207]\n" \
  "ds_read_b128 v[144:147], v249 offset:36864\n" \
  "v_mfma_f32_16x16x32_bf16 v[208:211], v[168:171], v[72:75], v[208:211]\n" \
  "ds_read_b128 v[148:151], v249 offset:37888\n" \
  "v_mfma_f32_16x16x32_bf16 v[212:215], v[172:175], v[72:75], v[212:215]\n" \
  "ds_read_b128 v[152:155], v249 offset:38912\n" \
  "v_mfma_f32_16x16x32_bf16 v[216:219], v[176:179], v[72:75], v[216:219]\n" \
  "ds_read_b128 v[156:159], v249 offset:39936\n" \
  "s_waitcnt vmcnt(9) lgkmcnt(10)\n" \
  "v_mfma_f32_16x16x32_bf16 v[200:203], v[180:183], v[76:79], v[200:203]\n" \
  "ds_read_b128 v[160:163], v249 offset:40960\n" \
  "v_mfma_f32_16x16x32_bf16 v[204:207], v[184:187], v[76:79], v[204:207]\n" \
  "ds_read_b128 v[164:167], v249 offset:41984\n" \
  "v_mfma_f32_16x16x32_bf16 v[208:211], v[188:191], v[76:79], v[208:211]\n" \
  "ds_read_b128 v[168:171], v249 offset:43008\n" \
  "v_mfma_f32_16x16x32_bf16 v[212:215], v[192:195], v[76:79], v[212:215]\n" \
  "ds_read_b128 v[172:175], v249 offset:44032\n" \
  "v_mfma_f32_16x16x32_bf16 v[216:219], v[196:199], v[76:79], v[216:219]\n" \
  "ds_read_b128 v[176:179], v249 offset:45056\n" \
  "s_waitcnt vmcnt(8) lgkmcnt(10)\n" \
  "v_mfma_f32_16x16x32_bf16 v[200:203], v[100:103], v[80:83], v[200:203]\n" \
  "ds_read_b128 v[180:183], v249 offset:46080\n" \
  "v_mfma_f32_16x16x32_bf16 v[204:207], v[104:107], v[80:83], v[204:207]\n" \
  "ds_read_b128 v[184:187], v249 offset:47104\n" \
  "v_mfma_f32_16x16x32_bf16 v[208:211], v[108:111], v[80:83], v[208:211]\n" \
  "ds_read_b128 v[188:191], v249 offset:48128\n" \
  "v_mfma_f32_16x16x32_bf16 v[212:215], v[112:115], v[80:83], v[212:215]\n" \
  "ds_read_b128 v[192:195], v249 offset:49152\n" \
  "v_mfma_f32_16x16x32_bf16 v[216:219], v[116:119], v[80:83], v[216:219]\n" \
  "ds_read_b128 v[196:199], v249 offset:50176\n" \
  "s_waitcnt vmcnt(6) lgkmcnt(10)\n" \
  "v_mfma_f32_16x16x32_bf16 v[200:203], v[120:123], v[84:87], v[200:203]\n" \
  "v_lshlrev_b32 v100, 16, v236\n" \
  "v_mfma_f32_16x16x32_bf16 v[204:207], v[124:127], v[84:87], v[204:207]\n" \
  "v_and_b32 v101, 0xffff0000, v236\n" \
  "v_mfma_f32_16x16x32_bf16 v[208:211], v[128:131], v[84:87], v[208:211]\n" \
  "v_lshlrev_b32 v102, 16, v237\n" \
  "v_mfma_f32_16x16x32_bf16 v[212:215], v[132:135], v[84:87], v[212:215]\n" \
  "v_and_b32 v103, 0xffff0000, v237\n" \
  "v_mfma_f32_16x16x32_bf16 v[216:219], v[136:139], v[84:87], v[216:219]\n" \
  "v_lshlrev_b32 v104, 16, v238\n" \
  "s_waitcnt vmcnt(4) lgkmcnt(0)\n" \
  "v_mfma_f32_16x16x32_bf16 v[200:203], v[140:143], v[88:91], v[200:203]\n" \
  "v_and_b32 v105, 0xffff0000, v238\n" \
  "v_mfma_f32_16x16x32_bf16 v[204:207], v[144:147], v[88:91], v[204:207]\n" \
  "v_lshlrev_b32 v106, 16, v239\n" \
  "v_mfma_f32_16x16x32_bf16 v[208:211], v[148:151], v[88:91], v[208:211]\n" \
  "v_and_b32 v107, 0xffff0000, v239\n" \
  "v_mfma_f32_16x16x32_bf16 v[212:215], v[152:155], v[88:91], v[212:215]\n" \
  "v_lshlrev_b32 v108, 16, v240\n" \
  "v_mfma_f32_16x16x32_bf16 v[216:219], v[156:159], v[88:91], v[216:219]\n" \
  "v_and_b32 v109, 0xffff0000, v240\n" \
  "s_waitcnt vmcnt(2)\n" \
  "v_mfma_f32_16x16x32_bf16 v[200:203], v[160:163], v[92:95], v[200:203]\n" \
  "v_lshlrev_b32 v110, 16, v241\n" \
  "v_mfma_f32_16x16x32_bf16 v[204:207], v[164:167], v[92:95], v[204:207]\n" \
  "v_and_b32 v111, 0xffff0000, v241\n" \
  "v_mfma_f32_16x16x32_bf16 v[208:211], v[168:171], v[92:95], v[208:211]\n" \
  "v_lshlrev_b32 v112, 16, v242\n" \
  "v_mfma_f32_16x16x32_bf16 v[212:215], v[172:175], v[92:95], v[212:215]\n" \
  "v_and_b32 v113, 0xffff0000, v242\n" \
  "v_mfma_f32_16x16x32_bf16 v[216:219], v[176:179], v[92:95], v[216:219]\n" \
  "v_lshlrev_b32 v114, 16, v243\n" \
  "s_waitcnt vmcnt(0)\n" \
  "v_mfma_f32_16x16x32_bf16 v[200:203], v[180:183], v[96:99], v[200:203]\n" \
  "v_and_b32 v115, 0xffff0000, v243\n" \
  "v_mfma_f32_16x16x32_bf16 v[204:207], v[184:187], v[96:99], v[204:207]\n" \
  "v_lshlrev_b32 v116, 16, v244\n" \
  "v_mfma_f32_16x16x32_bf16 v[208:211], v[188:191], v[96:99], v[208:211]\n" \
  "v_and_b32 v117, 0xffff0000, v244\n" \
  "v_mfma_f32_16x16x32_bf16 v[212:215], v[192:195], v[96:99], v[212:215]\n" \
  "v_lshlrev_b32 v118, 16, v245\n" \
  "v_mfma_f32_16x16x32_bf16 v[216:219], v[196:199], v[96:99], v[216:219]\n" \
  "v_and_b32 v119, 0xffff0000, v245\n" \
    \
  "v_pk_fma_f32 v[100:101], v[200:201], v[0:1], v[100:101]\n" \
  "v_pk_fma_f32 v[102:103], v[202:203], v[2:3], v[102:103]\n" \
  "v_pk_fma_f32 v[104:105], v[204:205], v[16:17], v[104:105]\n" \
  "v_pk_fma_f32 v[106:107], v[206:207], v[18:19], v[106:107]\n" \
  "v_pk_fma_f32 v[108:109], v[208:209], v[32:33], v[108:109]\n" \
  "v_pk_fma_f32 v[110:111], v[210:211], v[34:35], v[110:111]\n" \
  "v_pk_fma_f32 v[112:113], v[212:213], v[48:49], v[112:113]\n" \
  "v_pk_fma_f32 v[114:115], v[214:215], v[50:51], v[114:115]\n" \
  "v_pk_fma_f32 v[116:117], v[216:217], v[64:65], v[116:117]\n" \
  "v_pk_fma_f32 v[118:119], v[218:219], v[66:67], v[118:119]\n" \
  "v_cvt_pk_bf16_f32 v236, v100, v101\n" \
  "v_cvt_pk_bf16_f32 v237, v102, v103\n" \
  "v_cvt_pk_bf16_f32 v238, v104, v105\n" \
  "v_cvt_pk_bf16_f32 v239, v106, v107\n" \
  "v_cvt_pk_bf16_f32 v240, v108, v109\n" \
  "v_cvt_pk_bf16_f32 v241, v110, v111\n" \
  "v_cvt_pk_bf16_f32 v242, v112, v113\n" \
  "v_cvt_pk_bf16_f32 v243, v114, v115\n" \
  "v_cvt_pk_bf16_f32 v244, v116, v117\n" \
  "v_cvt_pk_bf16_f32 v245, v118, v119\n" \
  "ds_write_b64 v226, v[236:237] offset:0\n" \
  "ds_write_b64 v226, v[238:239] offset:8\n" \
  "ds_write_b64 v226, v[240:241] offset:16\n" \
  "ds_write_b64 v226, v[242:243] offset:24\n" \
  "ds_write_b64 v226, v[244:245] offset:32\n" \
  "ds_read_b128 v[0:3], v227 offset:0\n" \
  "ds_read_b128 v[4:7], v227 offset:1024\n" \
  "ds_read_b128 v[8:11], v227 offset:2048\n" \
  "s_waitcnt lgkmcnt(2)\n" \
  "buffer_store_dwordx4 v[0:3], v220, s[56:59], s80 offen\n" \
  "s_waitcnt lgkmcnt(1)\n" \
  "buffer_store_dwordx4 v[4:7], v221, s[56:59], s80 offen\n" \
  "s_waitcnt lgkmcnt(0)\n" \
  "s_mov_b32 exec_hi, 0\n" \
  "buffer_store_dwordx4 v[8:11], v222, s[56:59], s80 offen\n" \
  "s_endpgm\n" \
  ".Lqkv:\n" \
    \
  "s_mov_b32 s44, s24\n" \
  "s_and_b32 s45, s25, 0xffff\n" \
  "s_mov_b32 s46, 20152320\n" \
  "s_mov_b32 s47, 0x20000\n" \
  "s_lshl2_add_u32 s43, s31, s29\n" \
  "s_mul_i32 s35, s43, 52\n" \
  "s_and_b32 s35, s35, 0x300\n" \
  "s_lshl1_add_u32 s35, s30, s35\n" \
  "s_cmp_eq_u32 s33, 0\n" \
  "s_cselect_b32 s34, 1, 2\n" \
  "s_mul_i32 s49, s34, 2560\n" \
  "s_mov_b32 s34, 3211264\n" \
  "s_cselect_b32 s34, 589824, s34\n" \
  "s_mul_i32 s35, s35, s49\n" \
  "s_add_u32 s35, s35, s34\n" \
  "s_lshl_b32 s48, s30, 10\n" \
  "s_lshl_b32 s34, s29, 8\n" \
  "s_add_u32 s48, s48, s34\n" \
  "s_add_u32 s48, s48, 4096\n" \
  "s_cmp_eq_u32 s33, 3\n" \
  "s_cselect_b32 s48, s48, s35\n" \
  "s_add_u32 s49, s49, s48\n" \
  "s_lshl_b32 s51, s29, 6\n" \
  "s_lshl_b32 s58, s30, 11\n" \
  "s_lshl_b32 s34, s31, 7\n" \
  "s_add_u32 s58, s58, s34\n" \
  "s_lshl_b32 s34, s29, 5\n" \
  "s_add_u32 s58, s58, s34\n" \
  "s_add_u32 s58, s58, 32768\n" \
  "s_lshl_b32 s75, s43, 11\n" \
  "s_lshl_b32 s34, s30, 7\n" \
  "s_add_u32 s75, s75, s34\n" \
  "s_add_u32 s75, s75, 65536\n" \
  "s_and_b32 s27, s26, 7\n" \
  "s_bfe_u32 s34, s26, 0x20003\n" \
  "s_lshl2_add_u32 s27, s27, s34\n" \
  "s_lshr_b32 s70, s26, 5\n" \
  "s_lshr_b32 s34, s29, 1\n" \
  "s_lshl1_add_u32 s70, s70, s34\n" \
  "s_and_b32 s71, s29, 1\n" \
  "s_mul_i32 s61, s27, 163840\n" \
  "s_add_u32 s61, s61, 3211264\n" \
  "s_add_u32 s76, s24, s61\n" \
  "s_addc_u32 s77, s25, 0\n" \
  "s_and_b32 s77, s77, 0xffff\n" \
  "s_mov_b32 s79, 0x20000\n" \
  "s_mul_i32 s72, s71, 81920\n" \
  "s_add_u32 s78, s72, 81920\n" \
  "s_mul_i32 s60, s27, 81920\n" \
  "s_mul_i32 s34, s70, 5120\n" \
  "s_add_u32 s60, s60, s34\n" \
  "s_mul_i32 s34, s71, 2560\n" \
  "s_add_u32 s60, s60, s34\n" \
  "s_add_u32 s60, s60, 589824\n" \
  "s_mov_b32 s73, s60\n" \
  "s_xor_b32 s35, s71, 1\n" \
  "s_mul_i32 s74, s27, 81920\n" \
  "s_mul_i32 s34, s70, 5120\n" \
  "s_add_u32 s74, s74, s34\n" \
  "s_mul_i32 s34, s35, 2560\n" \
  "s_add_u32 s74, s74, s34\n" \
  "s_add_u32 s74, s74, 589824\n" \
  "s_lshl_b32 s35, s70, 6\n" \
  "s_lshl_b32 s34, s71, 5\n" \
  "s_add_u32 s35, s35, s34\n" \
  "s_add_u32 s35, s35, s27\n" \
  "s_mul_i32 s62, s35, 2560\n" \
  "s_add_u32 s62, s62, 14745600\n" \
  "s_lshl_b32 s63, s35, 7\n" \
  "s_add_u32 s63, s63, 327680\n" \
  "s_mul_i32 s68, s29, 49152\n" \
  "s_mul_i32 s34, s31, 192\n" \
  "s_add_u32 s68, s68, s34\n" \
  "s_add_u32 s68, s68, 983040\n" \
  "s_mul_i32 s69, s29, 3072\n" \
  "s_mov_b32 s64, s14\n" \
  "s_and_b32 s65, s15, 0xffff\n" \
  "s_mov_b32 s66, 1966080\n" \
  "s_mov_b32 s67, 0x20000\n" \
  "s_mul_i32 s35, s27, 20\n" \
  "s_lshr_b32 s80, s35, 5\n" \
  "s_add_u32 s35, s35, 19\n" \
  "s_lshr_b32 s81, s35, 5\n" \
  "s_sub_u32 s81, s81, s80\n" \
  "s_lshl_b32 s81, s81, 11\n" \
  "s_lshl_b32 s80, s80, 11\n" \
  "s_add_u32 s80, s80, 65536\n" \
  "s_lshl_b32 s71, s71, 10\n" \
  "s_lshl_b32 s70, s70, 7\n" \
  "s_lshl_b32 s35, s29, 2\n" \
  "s_add_u32 s35, s35, 157200\n" \
  "v_mov_b32 v244, s35\n" \
  "v_mov_b32 v152, 0x3f803f80\n" \
  "v_mov_b32 v153, 0x3f803f80\n" \
  "v_mov_b32 v154, 0x3f803f80\n" \
  "v_mov_b32 v155, 0x3f803f80\n" \
  "v_mov_b32 v160, 157184\n" \
  "v_mov_b64 v[72:73], 0\n" \
  "v_mov_b64 v[74:75], 0\n" \
  "ds_write_b128 v160, v[72:75]\n" \
  "ds_write_b128 v160, v[72:75] offset:16\n" \
  "s_waitcnt vmcnt(8)\n" \
  "v_xor_b32 v158, s51, v158\n" \
  "v_and_b32 v160, 1, v167\n" \
  "v_cmp_ne_u32 s[52:53], 0, v160\n" \
  "v_xor_b32 v159, s51, v159\n" \
  "v_and_b32 v160, 2, v167\n" \
  "v_cmp_ne_u32 s[54:55], 0, v160\n" \
  "v_mov_b32 v160, v156\n" \
  "v_mov_b32 v161, v157\n" \
  "v_mov_b32 v162, v158\n" \
  "v_mov_b32 v163, v159\n" \
  "s_mov_b32 s40, 0\n" \
  "s_mov_b32 s41, 1\n" \
  "s_mov_b32 s42, 0\n" \
  "s_cmp_eq_u32 s33, 2\n" \
  "s_waitcnt lgkmcnt(0)\n" \
  "s_barrier\n" \
  "s_cbranch_scc1 8f\n" \
    \
  "ds_read_b64_tr_b16 v[136:137], v162 offset:0\n" \
  "ds_read_b64_tr_b16 v[138:139], v162 offset:1024\n" \
  "ds_read_b64_tr_b16 v[140:141], v163 offset:0\n" \
  "ds_read_b64_tr_b16 v[142:143], v163 offset:1024\n" \
  "ds_read_b128 v[72:75], v160 offset:0\n" \
  "ds_read_b128 v[76:79], v161 offset:2048\n" \
  "ds_read_b128 v[80:83], v160 offset:4096\n" \
  "ds_read_b128 v[84:87], v161 offset:6144\n" \
  "ds_read_b128 v[88:91], v160 offset:8192\n" \
  "ds_read_b128 v[92:95], v161 offset:10240\n" \
  "ds_read_b128 v[96:99], v160 offset:12288\n" \
  "ds_read_b128 v[100:103], v161 offset:14336\n" \
  "s_waitcnt lgkmcnt(0)\n" \
  "v_mfma_f32_16x16x32_bf16 v[64:67], v[136:139], v[152:155], 0\n" \
  "ds_read_b64_tr_b16 v[144:145], v162 offset:8192\n" \
  "v_mfma_f32_16x16x32_bf16 v[68:71], v[140:143], v[152:155], 0\n" \
  "ds_read_b64_tr_b16 v[146:147], v162 offset:9216\n" \
  "v_mfma_f32_16x16x32_bf16 v[0:3], v[136:139], v[72:75], 0\n" \
  "ds_read_b64_tr_b16 v[148:149], v163 offset:8192\n" \
  "v_mfma_f32_16x16x32_bf16 v[32:35], v[140:143], v[72:75], 0\n" \
  "ds_read_b64_tr_b16 v[150:151], v163 offset:9216\n" \
  "v_mfma_f32_16x16x32_bf16 v[4:7], v[136:139], v[76:79], 0\n" \
  "ds_read_b128 v[104:107], v161 offset:0\n" \
  "v_mfma_f32_16x16x32_bf16 v[36:39], v[140:143], v[76:79], 0\n" \
  "ds_read_b128 v[108:111], v160 offset:2048\n" \
  "v_mfma_f32_16x16x32_bf16 v[8:11], v[136:139], v[80:83], 0\n" \
  "ds_read_b128 v[112:115], v161 offset:4096\n" \
  "v_mfma_f32_16x16x32_bf16 v[40:43], v[140:143], v[80:83], 0\n" \
  "ds_read_b128 v[116:119], v160 offset:6144\n" \
  "v_mfma_f32_16x16x32_bf16 v[12:15], v[136:139], v[84:87], 0\n" \
  "ds_read_b128 v[120:123], v161 offset:8192\n" \
  "v_mfma_f32_16x16x32_bf16 v[44:47], v[140:143], v[84:87], 0\n" \
  "ds_read_b128 v[124:127], v160 offset:10240\n" \
  "v_mfma_f32_16x16x32_bf16 v[16:19], v[136:139], v[88:91], 0\n" \
  "ds_read_b128 v[128:131], v161 offset:12288\n" \
  "v_mfma_f32_16x16x32_bf16 v[48:51], v[140:143], v[88:91], 0\n" \
  "ds_read_b128 v[132:135], v160 offset:14336\n" \
  "v_mfma_f32_16x16x32_bf16 v[20:23], v[136:139], v[92:95], 0\n" \
  "s_add_u32 s40, s40, 16384\n" \
  "s_and_b32 s40, s40, 0xffff\n" \
  "v_mfma_f32_16x16x32_bf16 v[52:55], v[140:143], v[92:95], 0\n" \
  "v_add_u32 v160, s40, v156\n" \
  "v_add_u32 v161, s40, v157\n" \
  "v_mfma_f32_16x16x32_bf16 v[24:27], v[136:139], v[96:99], 0\n" \
  "v_add_u32 v162, s40, v158\n" \
  "v_add_u32 v163, s40, v159\n" \
  "v_mfma_f32_16x16x32_bf16 v[56:59], v[140:143], v[96:99], 0\n" \
  "s_sub_u32 s41, s41, 1\n" \
  "v_mfma_f32_16x16x32_bf16 v[28:31], v[136:139], v[100:103], 0\n" \
  "v_mfma_f32_16x16x32_bf16 v[60:63], v[140:143], v[100:103], 0\n" \
  "s_waitcnt lgkmcnt(0)\n" \
  "1:\n" \
  "s_barrier\n" \
  "v_mfma_f32_16x16x32_bf16 v[64:67], v[144:147], v[152:155], v[64:67]\n" \
  "ds_read_b64_tr_b16 v[136:137], v162 offset:0\n" \
  "v_mfma_f32_16x16x32_bf16 v[68:71], v[148:151], v[152:155], v[68:71]\n" \
  "ds_read_b64_tr_b16 v[138:139], v162 offset:1024\n" \
  "v_mfma_f32_16x16x32_bf16 v[0:3], v[144:147], v[104:107], v[0:3]\n" \
  "ds_read_b64_tr_b16 v[140:141], v163 offset:0\n" \
  "v_mfma_f32_16x16x32_bf16 v[32:35], v[148:151], v[104:107], v[32:35]\n" \
  "ds_read_b64_tr_b16 v[142:143], v163 offset:1024\n" \
  "v_mfma_f32_16x16x32_bf16 v[4:7], v[144:147], v[108:111], v[4:7]\n" \
  "ds_read_b128 v[72:75], v160 offset:0\n" \
  "v_mfma_f32_16x16x32_bf16 v[36:39], v[148:151], v[108:111], v[36:39]\n" \
  "ds_read_b128 v[76:79], v161 offset:2048\n" \
  "v_mfma_f32_16x16x32_bf16 v[8:11], v[144:147], v[112:115], v[8:11]\n" \
  "ds_read_b128 v[80:83], v160 offset:4096\n" \
  "v_mfma_f32_16x16x32_bf16 v[40:43], v[148:151], v[112:115], v[40:43]\n" \
  "ds_read_b128 v[84:87], v161 offset:6144\n" \
  "v_mfma_f32_16x16x32_bf16 v[12:15], v[144:147], v[116:119], v[12:15]\n" \
  "ds_read_b128 v[88:91], v160 offset:8192\n" \
  "v_mfma_f32_16x16x32_bf16 v[44:47], v[148:151], v[116:119], v[44:47]\n" \
  "ds_read_b128 v[92:95], v161 offset:10240\n" \
  "v_mfma_f32_16x16x32_bf16 v[16:19], v[144:147], v[120:123], v[16:19]\n" \
  "ds_read_b128 v[96:99], v160 offset:12288\n" \
  "v_mfma_f32_16x16x32_bf16 v[48:51], v[148:151], v[120:123], v[48:51]\n" \
  "ds_read_b128 v[100:103], v161 offset:14336\n" \
  "v_mfma_f32_16x16x32_bf16 v[20:23], v[144:147], v[124:127], v[20:23]\n" \
  "v_mfma_f32_16x16x32_bf16 v[52:55], v[148:151], v[124:127], v[52:55]\n" \
  "v_mfma_f32_16x16x32_bf16 v[24:27], v[144:147], v[128:131], v[24:27]\n" \
  "v_mfma_f32_16x16x32_bf16 v[56:59], v[148:151], v[128:131], v[56:59]\n" \
  "v_mfma_f32_16x16x32_bf16 v[28:31], v[144:147], v[132:135], v[28:31]\n" \
  "v_mfma_f32_16x16x32_bf16 v[60:63], v[148:151], v[132:135], v[60:63]\n" \
  "s_waitcnt lgkmcnt(0)\n" \
  "v_mfma_f32_16x16x32_bf16 v[64:67], v[136:139], v[152:155], v[64:67]\n" \
  "ds_read_b64_tr_b16 v[144:145], v162 offset:8192\n" \
  "v_mfma_f32_16x16x32_bf16 v[68:71], v[140:143], v[152:155], v[68:71]\n" \
  "ds_read_b64_tr_b16 v[146:147], v162 offset:9216\n" \
  "v_mfma_f32_16x16x32_bf16 v[0:3], v[136:139], v[72:75], v[0:3]\n" \
  "ds_read_b64_tr_b16 v[148:149], v163 offset:8192\n" \
  "v_mfma_f32_16x16x32_bf16 v[32:35], v[140:143], v[72:75], v[32:35]\n" \
  "ds_read_b64_tr_b16 v[150:151], v163 offset:9216\n" \
  "v_mfma_f32_16x16x32_bf16 v[4:7], v[136:139], v[76:79], v[4:7]\n" \
  "ds_read_b128 v[104:107], v161 offset:0\n" \
  "v_mfma_f32_16x16x32_bf16 v[36:39], v[140:143], v[76:79], v[36:39]\n" \
  "ds_read_b128 v[108:111], v160 offset:2048\n" \
  "v_mfma_f32_16x16x32_bf16 v[8:11], v[136:139], v[80:83], v[8:11]\n" \
  "ds_read_b128 v[112:115], v161 offset:4096\n" \
  "v_mfma_f32_16x16x32_bf16 v[40:43], v[140:143], v[80:83], v[40:43]\n" \
  "ds_read_b128 v[116:119], v160 offset:6144\n" \
  "v_mfma_f32_16x16x32_bf16 v[12:15], v[136:139], v[84:87], v[12:15]\n" \
  "ds_read_b128 v[120:123], v161 offset:8192\n" \
  "v_mfma_f32_16x16x32_bf16 v[44:47], v[140:143], v[84:87], v[44:47]\n" \
  "ds_read_b128 v[124:127], v160 offset:10240\n" \
  "v_mfma_f32_16x16x32_bf16 v[16:19], v[136:139], v[88:91], v[16:19]\n" \
  "ds_read_b128 v[128:131], v161 offset:12288\n" \
  "v_mfma_f32_16x16x32_bf16 v[48:51], v[140:143], v[88:91], v[48:51]\n" \
  "ds_read_b128 v[132:135], v160 offset:14336\n" \
  "v_mfma_f32_16x16x32_bf16 v[20:23], v[136:139], v[92:95], v[20:23]\n" \
  "s_add_u32 s40, s40, 16384\n" \
  "s_and_b32 s40, s40, 0xffff\n" \
  "v_mfma_f32_16x16x32_bf16 v[52:55], v[140:143], v[92:95], v[52:55]\n" \
  "v_add_u32 v160, s40, v156\n" \
  "v_add_u32 v161, s40, v157\n" \
  "v_mfma_f32_16x16x32_bf16 v[24:27], v[136:139], v[96:99], v[24:27]\n" \
  "v_add_u32 v162, s40, v158\n" \
  "v_add_u32 v163, s40, v159\n" \
  "v_mfma_f32_16x16x32_bf16 v[56:59], v[140:143], v[96:99], v[56:59]\n" \
  "s_sub_u32 s41, s41, 1\n" \
  "v_mfma_f32_16x16x32_bf16 v[28:31], v[136:139], v[100:103], v[28:31]\n" \
  "v_mfma_f32_16x16x32_bf16 v[60:63], v[140:143], v[100:103], v[60:63]\n" \
  "s_waitcnt lgkmcnt(0)\n" \
  "s_cbranch_scc0 1b\n" \
  "s_cmp_lg_u32 s42, 0\n" \
  "s_cbranch_scc1 2f\n" \
    \
  "s_waitcnt vmcnt(1)\n" \
  "v_readfirstlane_b32 s36, v210\n" \
  "v_readfirstlane_b32 s37, v211\n" \
  "v_mov_b32 v216, 0\n" \
  "v_mov_b32 v217, 0\n" \
  "v_mov_b32 v218, 0\n" \
  "v_mov_b32 v219, 0\n" \
  "v_dot2c_f32_bf16 v216, v152, v200\n" \
  "v_dot2c_f32_bf16 v218, v200, v200\n" \
  "v_dot2c_f32_bf16 v217, v152, v201\n" \
  "v_dot2c_f32_bf16 v219, v201, v201\n" \
  "v_dot2c_f32_bf16 v216, v152, v202\n" \
  "v_dot2c_f32_bf16 v218, v202, v202\n" \
  "v_dot2c_f32_bf16 v217, v152, v203\n" \
  "v_dot2c_f32_bf16 v219, v203, v203\n" \
  "v_dot2c_f32_bf16 v216, v152, v204\n" \
  "v_dot2c_f32_bf16 v218, v204, v204\n" \
  "v_dot2c_f32_bf16 v217, v152, v205\n" \
  "v_dot2c_f32_bf16 v219, v205, v205\n" \
  "v_dot2c_f32_bf16 v216, v152, v206\n" \
  "v_dot2c_f32_bf16 v218, v206, v206\n" \
  "v_dot2c_f32_bf16 v217, v152, v207\n" \
  "v_dot2c_f32_bf16 v219, v207, v207\n" \
  "v_dot2c_f32_bf16 v216, v152, v208\n" \
  "v_dot2c_f32_bf16 v218, v208, v208\n" \
  "v_dot2c_f32_bf16 v217, v152, v209\n" \
  "v_dot2c_f32_bf16 v219, v209, v209\n" \
  "s_mul_i32 s38, s36, 0x5851f42d\n" \
  "s_mul_hi_u32 s39, s36, 0x4c957f2d\n" \
  "s_mul_i32 s37, s37, 0x4c957f2d\n" \
  "v_add_f32 v216, v216, v217\n" \
  "v_add_f32 v218, v218, v219\n" \
  "s_add_u32 s38, s38, s39\n" \
  "v_add_f32_dpp v216, v216, v216 quad_perm:[1,0,3,2] row_mask:0xf bank_mask:0xf bound_ctrl:1\n" \
  "v_add_f32_dpp v218, v218, v218 quad_perm:[1,0,3,2] row_mask:0xf bank_mask:0xf bound_ctrl:1\n" \
  "s_add_u32 s37, s37, s38\n" \
  "v_add_f32_dpp v216, v216, v216 quad_perm:[2,3,0,1] row_mask:0xf bank_mask:0xf bound_ctrl:1\n" \
  "v_add_f32_dpp v218, v218, v218 quad_perm:[2,3,0,1] row_mask:0xf bank_mask:0xf bound_ctrl:1\n" \
  "s_mul_i32 s36, s36, 0x4c957f2d\n" \
  "v_add_f32_dpp v216, v216, v216 row_half_mirror row_mask:0xf bank_mask:0xf bound_ctrl:1\n" \
  "v_add_f32_dpp v218, v218, v218 row_half_mirror row_mask:0xf bank_mask:0xf bound_ctrl:1\n" \
  "s_add_u32 s56, s36, 0xf767814f\n" \
  "v_add_f32_dpp v216, v216, v216 row_mirror row_mask:0xf bank_mask:0xf bound_ctrl:1\n" \
  "v_add_f32_dpp v218, v218, v218 row_mirror row_mask:0xf bank_mask:0xf bound_ctrl:1\n" \
  "s_addc_u32 s57, s37, 0x14057b7e\n" \
  "v_mov_b32 v217, v216\n" \
  "v_mov_b32 v219, v218\n" \
  "v_mbcnt_lo_u32_b32 v220, -1, 0\n" \
  "v_mbcnt_hi_u32_b32 v220, -1, v220\n" \
  "v_permlane16_swap_b32 v216, v217\n" \
  "v_permlane16_swap_b32 v218, v219\n" \
  "v_min_u32 v221, 47, v220\n" \
  "v_lshlrev_b32 v221, 3, v221\n" \
  "v_add_u32 v221, 157312, v221\n" \
  "v_add_f32 v216, v216, v217\n" \
  "v_add_f32 v217, v218, v219\n" \
  "v_mul_f32 v218, 0xbacccccd, v216\n" \
  "v_mul_f32 v217, 0x3acccccd, v217\n" \
  "v_fma_f32 v216, -v218, v218, v217\n" \
  "v_max_f32 v216, 0, v216\n" \
  "v_add_f32 v216, 0x3727c5ac, v216\n" \
  "v_rsq_f32 v216, v216\n" \
  "v_mov_b32 v217, s56\n" \
  "v_mov_b32 v219, s57\n" \
  "v_lshrrev_b32 v220, 1, v220\n" \
  "v_lshlrev_b32 v222, 16, v212\n" \
  "v_and_b32 v223, 0xffff0000, v212\n" \
  "ds_write_b64 v221, v[222:223]\n" \
  "v_mov_b32 v221, 157248\n" \
  "s_mov_b32 exec_lo, 1\n" \
  "s_mov_b32 exec_hi, 1\n" \
  "buffer_store_dwordx4 v[216:219], v220, s[44:47], s58 offen sc1\n" \
  "ds_write2_b32 v221, v217, v219 offset0:0 offset1:1\n" \
  "s_mov_b64 exec, -1\n" \
  "s_waitcnt lgkmcnt(0)\n" \
  "s_mov_b32 s42, 1\n" \
  "s_mov_b32 s41, 7\n" \
  "s_branch 1b\n" \
  "2:\n" \
  "s_barrier\n" \
    \
  "v_mfma_f32_16x16x32_bf16 v[64:67], v[144:147], v[152:155], v[64:67]\n" \
  "ds_read2_b32 v[72:73], v166 offset0:0 offset1:128\n" \
  "v_mfma_f32_16x16x32_bf16 v[68:71], v[148:151], v[152:155], v[68:71]\n" \
  "ds_read2_b32 v[74:75], v166 offset0:16 offset1:144\n" \
  "v_mfma_f32_16x16x32_bf16 v[0:3], v[144:147], v[104:107], v[0:3]\n" \
  "ds_read2_b32 v[76:77], v166 offset0:32 offset1:160\n" \
  "v_mfma_f32_16x16x32_bf16 v[32:35], v[148:151], v[104:107], v[32:35]\n" \
  "ds_read2_b32 v[78:79], v166 offset0:48 offset1:176\n" \
  "v_mfma_f32_16x16x32_bf16 v[4:7], v[144:147], v[108:111], v[4:7]\n" \
  "ds_read2_b32 v[80:81], v166 offset0:64 offset1:192\n" \
  "v_mfma_f32_16x16x32_bf16 v[36:39], v[148:151], v[108:111], v[36:39]\n" \
  "ds_read2_b32 v[82:83], v166 offset0:80 offset1:208\n" \
  "v_mfma_f32_16x16x32_bf16 v[8:11], v[144:147], v[112:115], v[8:11]\n" \
  "ds_read2_b32 v[84:85], v166 offset0:96 offset1:224\n" \
  "v_mfma_f32_16x16x32_bf16 v[40:43], v[148:151], v[112:115], v[40:43]\n" \
  "ds_read2_b32 v[86:87], v166 offset0:112 offset1:240\n" \
  "v_mfma_f32_16x16x32_bf16 v[12:15], v[144:147], v[116:119], v[12:15]\n" \
  "v_mfma_f32_16x16x32_bf16 v[44:47], v[148:151], v[116:119], v[44:47]\n" \
  "v_mfma_f32_16x16x32_bf16 v[16:19], v[144:147], v[120:123], v[16:19]\n" \
  "v_mfma_f32_16x16x32_bf16 v[48:51], v[148:151], v[120:123], v[48:51]\n" \
  "v_mfma_f32_16x16x32_bf16 v[20:23], v[144:147], v[124:127], v[20:23]\n" \
  "v_mfma_f32_16x16x32_bf16 v[52:55], v[148:151], v[124:127], v[52:55]\n" \
  "v_mfma_f32_16x16x32_bf16 v[24:27], v[144:147], v[128:131], v[24:27]\n" \
  "v_mfma_f32_16x16x32_bf16 v[56:59], v[148:151], v[128:131], v[56:59]\n" \
  "v_mfma_f32_16x16x32_bf16 v[28:31], v[144:147], v[132:135], v[28:31]\n" \
  "v_mfma_f32_16x16x32_bf16 v[60:63], v[148:151], v[132:135], v[60:63]\n" \
  "s_cmp_eq_u32 s33, 3\n" \
  "s_waitcnt lgkmcnt(0)\n" \
  "s_cbranch_scc1 3f\n" \
  "v_pk_fma_f32 v[0:1], v[64:65], v[72:73], v[0:1] op_sel_hi:[1,0,1]\n" \
  "v_pk_fma_f32 v[2:3], v[66:67], v[72:73], v[2:3] op_sel_hi:[1,0,1]\n" \
  "v_pk_fma_f32 v[8:9], v[64:65], v[76:77], v[8:9] op_sel_hi:[1,0,1]\n" \
  "v_pk_fma_f32 v[10:11], v[66:67], v[76:77], v[10:11] op_sel_hi:[1,0,1]\n" \
  "v_pk_mul_f32 v[0:1], v[0:1], v[72:73] op_sel:[0,1]\n" \
  "v_pk_mul_f32 v[2:3], v[2:3], v[72:73] op_sel:[0,1]\n" \
  "v_pk_mul_f32 v[8:9], v[8:9], v[76:77] op_sel:[0,1]\n" \
  "v_pk_mul_f32 v[10:11], v[10:11], v[76:77] op_sel:[0,1]\n" \
  "v_cvt_pk_bf16_f32 v104, v0, v1\n" \
  "v_cvt_pk_bf16_f32 v105, v2, v3\n" \
  "v_cvt_pk_bf16_f32 v106, v8, v9\n" \
  "v_cvt_pk_bf16_f32 v107, v10, v11\n" \
  "v_pk_fma_f32 v[4:5], v[64:65], v[74:75], v[4:5] op_sel_hi:[1,0,1]\n" \
  "v_pk_fma_f32 v[6:7], v[66:67], v[74:75], v[6:7] op_sel_hi:[1,0,1]\n" \
  "v_pk_fma_f32 v[12:13], v[64:65], v[78:79], v[12:13] op_sel_hi:[1,0,1]\n" \
  "v_pk_fma_f32 v[14:15], v[66:67], v[78:79], v[14:15] op_sel_hi:[1,0,1]\n" \
  "v_pk_mul_f32 v[4:5], v[4:5], v[74:75] op_sel:[0,1]\n" \
  "v_pk_mul_f32 v[6:7], v[6:7], v[74:75] op_sel:[0,1]\n" \
  "v_pk_mul_f32 v[12:13], v[12:13], v[78:79] op_sel:[0,1]\n" \
  "v_pk_mul_f32 v[14:15], v[14:15], v[78:79] op_sel:[0,1]\n" \
  "v_cvt_pk_bf16_f32 v108, v4, v5\n" \
  "v_cvt_pk_bf16_f32 v109, v6, v7\n" \
  "v_cvt_pk_bf16_f32 v110, v12, v13\n" \
  "v_cvt_pk_bf16_f32 v111, v14, v15\n" \
  "v_pk_fma_f32 v[16:17], v[64:65], v[80:81], v[16:17] op_sel_hi:[1,0,1]\n" \
  "v_pk_fma_f32 v[18:19], v[66:67], v[80:81], v[18:19] op_sel_hi:[1,0,1]\n" \
  "v_pk_fma_f32 v[24:25], v[64:65], v[84:85], v[24:25] op_sel_hi:[1,0,1]\n" \
  "v_pk_fma_f32 v[26:27], v[66:67], v[84:85], v[26:27] op_sel_hi:[1,0,1]\n" \
  "v_pk_mul_f32 v[16:17], v[16:17], v[80:81] op_sel:[0,1]\n" \
  "v_pk_mul_f32 v[18:19], v[18:19], v[80:81] op_sel:[0,1]\n" \
  "v_pk_mul_f32 v[24:25], v[24:25], v[84:85] op_sel:[0,1]\n" \
  "v_pk_mul_f32 v[26:27], v[26:27], v[84:85] op_sel:[0,1]\n" \
  "v_cvt_pk_bf16_f32 v112, v16, v17\n" \
  "v_cvt_pk_bf16_f32 v113, v18, v19\n" \
  "v_cvt_pk_bf16_f32 v114, v24, v25\n" \
  "v_cvt_pk_bf16_f32 v115, v26, v27\n" \
  "v_pk_fma_f32 v[20:21], v[64:65], v[82:83], v[20:21] op_sel_hi:[1,0,1]\n" \
  "v_pk_fma_f32 v[22:23], v[66:67], v[82:83], v[22:23] op_sel_hi:[1,0,1]\n" \
  "v_pk_fma_f32 v[28:29], v[64:65], v[86:87], v[28:29] op_sel_hi:[1,0,1]\n" \
  "v_pk_fma_f32 v[30:31], v[66:67], v[86:87], v[30:31] op_sel_hi:[1,0,1]\n" \
  "v_pk_mul_f32 v[20:21], v[20:21], v[82:83] op_sel:[0,1]\n" \
  "v_pk_mul_f32 v[22:23], v[22:23], v[82:83] op_sel:[0,1]\n" \
  "v_pk_mul_f32 v[28:29], v[28:29], v[86:87] op_sel:[0,1]\n" \
  "v_pk_mul_f32 v[30:31], v[30:31], v[86:87] op_sel:[0,1]\n" \
  "v_cvt_pk_bf16_f32 v116, v20, v21\n" \
  "v_cvt_pk_bf16_f32 v117, v22, v23\n" \
  "v_cvt_pk_bf16_f32 v118, v28, v29\n" \
  "v_cvt_pk_bf16_f32 v119, v30, v31\n" \
  "s_mov_b64 exec, s[52:53]\n" \
  "v_swap_b32 v104, v106\n" \
  "v_swap_b32 v105, v107\n" \
  "v_swap_b32 v108, v110\n" \
  "v_swap_b32 v109, v111\n" \
  "v_swap_b32 v112, v114\n" \
  "v_swap_b32 v113, v115\n" \
  "v_swap_b32 v116, v118\n" \
  "v_swap_b32 v117, v119\n" \
  "s_mov_b64 exec, -1\n" \
  "buffer_store_dwordx4 v[104:107], v164, s[44:47], s48 offen offset:0 sc1\n" \
  "buffer_store_dwordx4 v[108:111], v164, s[44:47], s48 offen offset:256 sc1\n" \
  "buffer_store_dwordx4 v[112:115], v164, s[44:47], s49 offen offset:0 sc1\n" \
  "buffer_store_dwordx4 v[116:119], v164, s[44:47], s49 offen offset:256 sc1\n" \
  "v_pk_fma_f32 v[32:33], v[68:69], v[72:73], v[32:33] op_sel_hi:[1,0,1]\n" \
  "v_pk_fma_f32 v[34:35], v[70:71], v[72:73], v[34:35] op_sel_hi:[1,0,1]\n" \
  "v_pk_fma_f32 v[40:41], v[68:69], v[76:77], v[40:41] op_sel_hi:[1,0,1]\n" \
  "v_pk_fma_f32 v[42:43], v[70:71], v[76:77], v[42:43] op_sel_hi:[1,0,1]\n" \
  "v_pk_mul_f32 v[32:33], v[32:33], v[72:73] op_sel:[0,1]\n" \
  "v_pk_mul_f32 v[34:35], v[34:35], v[72:73] op_sel:[0,1]\n" \
  "v_pk_mul_f32 v[40:41], v[40:41], v[76:77] op_sel:[0,1]\n" \
  "v_pk_mul_f32 v[42:43], v[42:43], v[76:77] op_sel:[0,1]\n" \
  "v_cvt_pk_bf16_f32 v120, v32, v33\n" \
  "v_cvt_pk_bf16_f32 v121, v34, v35\n" \
  "v_cvt_pk_bf16_f32 v122, v40, v41\n" \
  "v_cvt_pk_bf16_f32 v123, v42, v43\n" \
  "v_pk_fma_f32 v[36:37], v[68:69], v[74:75], v[36:37] op_sel_hi:[1,0,1]\n" \
  "v_pk_fma_f32 v[38:39], v[70:71], v[74:75], v[38:39] op_sel_hi:[1,0,1]\n" \
  "v_pk_fma_f32 v[44:45], v[68:69], v[78:79], v[44:45] op_sel_hi:[1,0,1]\n" \
  "v_pk_fma_f32 v[46:47], v[70:71], v[78:79], v[46:47] op_sel_hi:[1,0,1]\n" \
  "v_pk_mul_f32 v[36:37], v[36:37], v[74:75] op_sel:[0,1]\n" \
  "v_pk_mul_f32 v[38:39], v[38:39], v[74:75] op_sel:[0,1]\n" \
  "v_pk_mul_f32 v[44:45], v[44:45], v[78:79] op_sel:[0,1]\n" \
  "v_pk_mul_f32 v[46:47], v[46:47], v[78:79] op_sel:[0,1]\n" \
  "v_cvt_pk_bf16_f32 v124, v36, v37\n" \
  "v_cvt_pk_bf16_f32 v125, v38, v39\n" \
  "v_cvt_pk_bf16_f32 v126, v44, v45\n" \
  "v_cvt_pk_bf16_f32 v127, v46, v47\n" \
  "v_pk_fma_f32 v[48:49], v[68:69], v[80:81], v[48:49] op_sel_hi:[1,0,1]\n" \
  "v_pk_fma_f32 v[50:51], v[70:71], v[80:81], v[50:51] op_sel_hi:[1,0,1]\n" \
  "v_pk_fma_f32 v[56:57], v[68:69], v[84:85], v[56:57] op_sel_hi:[1,0,1]\n" \
  "v_pk_fma_f32 v[58:59], v[70:71], v[84:85], v[58:59] op_sel_hi:[1,0,1]\n" \
  "v_pk_mul_f32 v[48:49], v[48:49], v[80:81] op_sel:[0,1]\n" \
  "v_pk_mul_f32 v[50:51], v[50:51], v[80:81] op_sel:[0,1]\n" \
  "v_pk_mul_f32 v[56:57], v[56:57], v[84:85] op_sel:[0,1]\n" \
  "v_pk_mul_f32 v[58:59], v[58:59], v[84:85] op_sel:[0,1]\n" \
  "v_cvt_pk_bf16_f32 v128, v48, v49\n" \
  "v_cvt_pk_bf16_f32 v129, v50, v51\n" \
  "v_cvt_pk_bf16_f32 v130, v56, v57\n" \
  "v_cvt_pk_bf16_f32 v131, v58, v59\n" \
  "v_pk_fma_f32 v[52:53], v[68:69], v[82:83], v[52:53] op_sel_hi:[1,0,1]\n" \
  "v_pk_fma_f32 v[54:55], v[70:71], v[82:83], v[54:55] op_sel_hi:[1,0,1]\n" \
  "v_pk_fma_f32 v[60:61], v[68:69], v[86:87], v[60:61] op_sel_hi:[1,0,1]\n" \
  "v_pk_fma_f32 v[62:63], v[70:71], v[86:87], v[62:63] op_sel_hi:[1,0,1]\n" \
  "v_pk_mul_f32 v[52:53], v[52:53], v[82:83] op_sel:[0,1]\n" \
  "v_pk_mul_f32 v[54:55], v[54:55], v[82:83] op_sel:[0,1]\n" \
  "v_pk_mul_f32 v[60:61], v[60:61], v[86:87] op_sel:[0,1]\n" \
  "v_pk_mul_f32 v[62:63], v[62:63], v[86:87] op_sel:[0,1]\n" \
  "v_cvt_pk_bf16_f32 v132, v52, v53\n" \
  "v_cvt_pk_bf16_f32 v133, v54, v55\n" \
  "v_cvt_pk_bf16_f32 v134, v60, v61\n" \
  "v_cvt_pk_bf16_f32 v135, v62, v63\n" \
  "s_mov_b64 exec, s[54:55]\n" \
  "v_swap_b32 v120, v122\n" \
  "v_swap_b32 v121, v123\n" \
  "v_swap_b32 v124, v126\n" \
  "v_swap_b32 v125, v127\n" \
  "v_swap_b32 v128, v130\n" \
  "v_swap_b32 v129, v131\n" \
  "v_swap_b32 v132, v134\n" \
  "v_swap_b32 v133, v135\n" \
  "s_mov_b64 exec, -1\n" \
  "buffer_store_dwordx4 v[120:123], v165, s[44:47], s48 offen offset:0 sc1\n" \
  "buffer_store_dwordx4 v[124:127], v165, s[44:47], s48 offen offset:256 sc1\n" \
  "buffer_store_dwordx4 v[128:131], v165, s[44:47], s49 offen offset:0 sc1\n" \
  "buffer_store_dwordx4 v[132:135], v165, s[44:47], s49 offen offset:256 sc1\n" \
  "s_branch 9f\n" \
    \
  "3:\n" \
  "s_mov_b32 exec_lo, 0x00010001\n" \
  "s_mov_b32 exec_hi, 0x00010001\n" \
  "v_mov_b32 v104, v64\n" \
  "v_mov_b32 v105, s56\n" \
  "v_mov_b32 v106, v65\n" \
  "v_mov_b32 v107, s57\n" \
  "buffer_store_dwordx4 v[104:107], v164, s[44:47], s48 offen offset:0 sc1\n" \
  "v_mov_b32 v108, v66\n" \
  "v_mov_b32 v109, s56\n" \
  "v_mov_b32 v110, v67\n" \
  "v_mov_b32 v111, s57\n" \
  "buffer_store_dwordx4 v[108:111], v164, s[44:47], s48 offen offset:16 sc1\n" \
  "v_mov_b32 v112, v68\n" \
  "v_mov_b32 v113, s56\n" \
  "v_mov_b32 v114, v69\n" \
  "v_mov_b32 v115, s57\n" \
  "buffer_store_dwordx4 v[112:115], v165, s[44:47], s48 offen offset:0 sc1\n" \
  "v_mov_b32 v116, v70\n" \
  "v_mov_b32 v117, s56\n" \
  "v_mov_b32 v118, v71\n" \
  "v_mov_b32 v119, s57\n" \
  "buffer_store_dwordx4 v[116:119], v165, s[44:47], s48 offen offset:16 sc1\n" \
  "s_mov_b64 exec, -1\n" \
  "s_branch 9f\n" \
  "8:\n" \
    \
  "ds_read_b64_tr_b16 v[136:137], v162 offset:0\n" \
  "ds_read_b64_tr_b16 v[138:139], v162 offset:1024\n" \
  "ds_read_b64_tr_b16 v[140:141], v163 offset:0\n" \
  "ds_read_b64_tr_b16 v[142:143], v163 offset:1024\n" \
  "ds_read_b128 v[72:75], v160 offset:0\n" \
  "ds_read_b128 v[76:79], v161 offset:512\n" \
  "ds_read_b128 v[80:83], v160 offset:4096\n" \
  "ds_read_b128 v[84:87], v161 offset:4608\n" \
  "ds_read_b128 v[88:91], v160 offset:8192\n" \
  "ds_read_b128 v[92:95], v161 offset:8704\n" \
  "ds_read_b128 v[96:99], v160 offset:12288\n" \
  "ds_read_b128 v[100:103], v161 offset:12800\n" \
  "s_waitcnt lgkmcnt(0)\n" \
  "v_mfma_f32_16x16x32_bf16 v[64:67], v[152:155], v[136:139], 0\n" \
  "ds_read_b64_tr_b16 v[144:145], v162 offset:8192\n" \
  "v_mfma_f32_16x16x32_bf16 v[68:71], v[152:155], v[140:143], 0\n" \
  "ds_read_b64_tr_b16 v[146:147], v162 offset:9216\n" \
  "v_mfma_f32_16x16x32_bf16 v[0:3], v[72:75], v[136:139], 0\n" \
  "ds_read_b64_tr_b16 v[148:149], v163 offset:8192\n" \
  "v_mfma_f32_16x16x32_bf16 v[32:35], v[72:75], v[140:143], 0\n" \
  "ds_read_b64_tr_b16 v[150:151], v163 offset:9216\n" \
  "v_mfma_f32_16x16x32_bf16 v[4:7], v[76:79], v[136:139], 0\n" \
  "ds_read_b128 v[104:107], v161 offset:0\n" \
  "v_mfma_f32_16x16x32_bf16 v[36:39], v[76:79], v[140:143], 0\n" \
  "ds_read_b128 v[108:111], v160 offset:512\n" \
  "v_mfma_f32_16x16x32_bf16 v[8:11], v[80:83], v[136:139], 0\n" \
  "ds_read_b128 v[112:115], v161 offset:4096\n" \
  "v_mfma_f32_16x16x32_bf16 v[40:43], v[80:83], v[140:143], 0\n" \
  "ds_read_b128 v[116:119], v160 offset:4608\n" \
  "v_mfma_f32_16x16x32_bf16 v[12:15], v[84:87], v[136:139], 0\n" \
  "ds_read_b128 v[120:123], v161 offset:8192\n" \
  "v_mfma_f32_16x16x32_bf16 v[44:47], v[84:87], v[140:143], 0\n" \
  "ds_read_b128 v[124:127], v160 offset:8704\n" \
  "v_mfma_f32_16x16x32_bf16 v[16:19], v[88:91], v[136:139], 0\n" \
  "ds_read_b128 v[128:131], v161 offset:12288\n" \
  "v_mfma_f32_16x16x32_bf16 v[48:51], v[88:91], v[140:143], 0\n" \
  "ds_read_b128 v[132:135], v160 offset:12800\n" \
  "v_mfma_f32_16x16x32_bf16 v[20:23], v[92:95], v[136:139], 0\n" \
  "s_add_u32 s40, s40, 16384\n" \
  "s_and_b32 s40, s40, 0xffff\n" \
  "v_mfma_f32_16x16x32_bf16 v[52:55], v[92:95], v[140:143], 0\n" \
  "v_add_u32 v160, s40, v156\n" \
  "v_add_u32 v161, s40, v157\n" \
  "v_mfma_f32_16x16x32_bf16 v[24:27], v[96:99], v[136:139], 0\n" \
  "v_add_u32 v162, s40, v158\n" \
  "v_add_u32 v163, s40, v159\n" \
  "v_mfma_f32_16x16x32_bf16 v[56:59], v[96:99], v[140:143], 0\n" \
  "s_sub_u32 s41, s41, 1\n" \
  "v_mfma_f32_16x16x32_bf16 v[28:31], v[100:103], v[136:139], 0\n" \
  "v_mfma_f32_16x16x32_bf16 v[60:63], v[100:103], v[140:143], 0\n" \
  "s_waitcnt lgkmcnt(0)\n" \
  "1:\n" \
  "s_barrier\n" \
  "v_mfma_f32_16x16x32_bf16 v[64:67], v[152:155], v[144:147], v[64:67]\n" \
  "ds_read_b64_tr_b16 v[136:137], v162 offset:0\n" \
  "v_mfma_f32_16x16x32_bf16 v[68:71], v[152:155], v[148:151], v[68:71]\n" \
  "ds_read_b64_tr_b16 v[138:139], v162 offset:1024\n" \
  "v_mfma_f32_16x16x32_bf16 v[0:3], v[104:107], v[144:147], v[0:3]\n" \
  "ds_read_b64_tr_b16 v[140:141], v163 offset:0\n" \
  "v_mfma_f32_16x16x32_bf16 v[32:35], v[104:107], v[148:151], v[32:35]\n" \
  "ds_read_b64_tr_b16 v[142:143], v163 offset:1024\n" \
  "v_mfma_f32_16x16x32_bf16 v[4:7], v[108:111], v[144:147], v[4:7]\n" \
  "ds_read_b128 v[72:75], v160 offset:0\n" \
  "v_mfma_f32_16x16x32_bf16 v[36:39], v[108:111], v[148:151], v[36:39]\n" \
  "ds_read_b128 v[76:79], v161 offset:512\n" \
  "v_mfma_f32_16x16x32_bf16 v[8:11], v[112:115], v[144:147], v[8:11]\n" \
  "ds_read_b128 v[80:83], v160 offset:4096\n" \
  "v_mfma_f32_16x16x32_bf16 v[40:43], v[112:115], v[148:151], v[40:43]\n" \
  "ds_read_b128 v[84:87], v161 offset:4608\n" \
  "v_mfma_f32_16x16x32_bf16 v[12:15], v[116:119], v[144:147], v[12:15]\n" \
  "ds_read_b128 v[88:91], v160 offset:8192\n" \
  "v_mfma_f32_16x16x32_bf16 v[44:47], v[116:119], v[148:151], v[44:47]\n" \
  "ds_read_b128 v[92:95], v161 offset:8704\n" \
  "v_mfma_f32_16x16x32_bf16 v[16:19], v[120:123], v[144:147], v[16:19]\n" \
  "ds_read_b128 v[96:99], v160 offset:12288\n" \
  "v_mfma_f32_16x16x32_bf16 v[48:51], v[120:123], v[148:151], v[48:51]\n" \
  "ds_read_b128 v[100:103], v161 offset:12800\n" \
  "v_mfma_f32_16x16x32_bf16 v[20:23], v[124:127], v[144:147], v[20:23]\n" \
  "v_mfma_f32_16x16x32_bf16 v[52:55], v[124:127], v[148:151], v[52:55]\n" \
  "v_mfma_f32_16x16x32_bf16 v[24:27], v[128:131], v[144:147], v[24:27]\n" \
  "v_mfma_f32_16x16x32_bf16 v[56:59], v[128:131], v[148:151], v[56:59]\n" \
  "v_mfma_f32_16x16x32_bf16 v[28:31], v[132:135], v[144:147], v[28:31]\n" \
  "v_mfma_f32_16x16x32_bf16 v[60:63], v[132:135], v[148:151], v[60:63]\n" \
  "s_waitcnt lgkmcnt(0)\n" \
  "v_mfma_f32_16x16x32_bf16 v[64:67], v[152:155], v[136:139], v[64:67]\n" \
  "ds_read_b64_tr_b16 v[144:145], v162 offset:8192\n" \
  "v_mfma_f32_16x16x32_bf16 v[68:71], v[152:155], v[140:143], v[68:71]\n" \
  "ds_read_b64_tr_b16 v[146:147], v162 offset:9216\n" \
  "v_mfma_f32_16x16x32_bf16 v[0:3], v[72:75], v[136:139], v[0:3]\n" \
  "ds_read_b64_tr_b16 v[148:149], v163 offset:8192\n" \
  "v_mfma_f32_16x16x32_bf16 v[32:35], v[72:75], v[140:143], v[32:35]\n" \
  "ds_read_b64_tr_b16 v[150:151], v163 offset:9216\n" \
  "v_mfma_f32_16x16x32_bf16 v[4:7], v[76:79], v[136:139], v[4:7]\n" \
  "ds_read_b128 v[104:107], v161 offset:0\n" \
  "v_mfma_f32_16x16x32_bf16 v[36:39], v[76:79], v[140:143], v[36:39]\n" \
  "ds_read_b128 v[108:111], v160 offset:512\n" \
  "v_mfma_f32_16x16x32_bf16 v[8:11], v[80:83], v[136:139], v[8:11]\n" \
  "ds_read_b128 v[112:115], v161 offset:4096\n" \
  "v_mfma_f32_16x16x32_bf16 v[40:43], v[80:83], v[140:143], v[40:43]\n" \
  "ds_read_b128 v[116:119], v160 offset:4608\n" \
  "v_mfma_f32_16x16x32_bf16 v[12:15], v[84:87], v[136:139], v[12:15]\n" \
  "ds_read_b128 v[120:123], v161 offset:8192\n" \
  "v_mfma_f32_16x16x32_bf16 v[44:47], v[84:87], v[140:143], v[44:47]\n" \
  "ds_read_b128 v[124:127], v160 offset:8704\n" \
  "v_mfma_f32_16x16x32_bf16 v[16:19], v[88:91], v[136:139], v[16:19]\n" \
  "ds_read_b128 v[128:131], v161 offset:12288\n" \
  "v_mfma_f32_16x16x32_bf16 v[48:51], v[88:91], v[140:143], v[48:51]\n" \
  "ds_read_b128 v[132:135], v160 offset:12800\n" \
  "v_mfma_f32_16x16x32_bf16 v[20:23], v[92:95], v[136:139], v[20:23]\n" \
  "s_add_u32 s40, s40, 16384\n" \
  "s_and_b32 s40, s40, 0xffff\n" \
  "v_mfma_f32_16x16x32_bf16 v[52:55], v[92:95], v[140:143], v[52:55]\n" \
  "v_add_u32 v160, s40, v156\n" \
  "v_add_u32 v161, s40, v157\n" \
  "v_mfma_f32_16x16x32_bf16 v[24:27], v[96:99], v[136:139], v[24:27]\n" \
  "v_add_u32 v162, s40, v158\n" \
  "v_add_u32 v163, s40, v159\n" \
  "v_mfma_f32_16x16x32_bf16 v[56:59], v[96:99], v[140:143], v[56:59]\n" \
  "s_sub_u32 s41, s41, 1\n" \
  "v_mfma_f32_16x16x32_bf16 v[28:31], v[100:103], v[136:139], v[28:31]\n" \
  "v_mfma_f32_16x16x32_bf16 v[60:63], v[100:103], v[140:143], v[60:63]\n" \
  "s_waitcnt lgkmcnt(0)\n" \
  "s_cbranch_scc0 1b\n" \
  "s_cmp_lg_u32 s42, 0\n" \
  "s_cbranch_scc1 2f\n" \
    \
  "s_waitcnt vmcnt(1)\n" \
  "v_readfirstlane_b32 s36, v210\n" \
  "v_readfirstlane_b32 s37, v211\n" \
  "v_mov_b32 v216, 0\n" \
  "v_mov_b32 v217, 0\n" \
  "v_mov_b32 v218, 0\n" \
  "v_mov_b32 v219, 0\n" \
  "v_dot2c_f32_bf16 v216, v152, v200\n" \
  "v_dot2c_f32_bf16 v218, v200, v200\n" \
  "v_dot2c_f32_bf16 v217, v152, v201\n" \
  "v_dot2c_f32_bf16 v219, v201, v201\n" \
  "v_dot2c_f32_bf16 v216, v152, v202\n" \
  "v_dot2c_f32_bf16 v218, v202, v202\n" \
  "v_dot2c_f32_bf16 v217, v152, v203\n" \
  "v_dot2c_f32_bf16 v219, v203, v203\n" \
  "v_dot2c_f32_bf16 v216, v152, v204\n" \
  "v_dot2c_f32_bf16 v218, v204, v204\n" \
  "v_dot2c_f32_bf16 v217, v152, v205\n" \
  "v_dot2c_f32_bf16 v219, v205, v205\n" \
  "v_dot2c_f32_bf16 v216, v152, v206\n" \
  "v_dot2c_f32_bf16 v218, v206, v206\n" \
  "v_dot2c_f32_bf16 v217, v152, v207\n" \
  "v_dot2c_f32_bf16 v219, v207, v207\n" \
  "v_dot2c_f32_bf16 v216, v152, v208\n" \
  "v_dot2c_f32_bf16 v218, v208, v208\n" \
  "v_dot2c_f32_bf16 v217, v152, v209\n" \
  "v_dot2c_f32_bf16 v219, v209, v209\n" \
  "s_mul_i32 s38, s36, 0x5851f42d\n" \
  "s_mul_hi_u32 s39, s36, 0x4c957f2d\n" \
  "s_mul_i32 s37, s37, 0x4c957f2d\n" \
  "v_add_f32 v216, v216, v217\n" \
  "v_add_f32 v218, v218, v219\n" \
  "s_add_u32 s38, s38, s39\n" \
  "v_add_f32_dpp v216, v216, v216 quad_perm:[1,0,3,2] row_mask:0xf bank_mask:0xf bound_ctrl:1\n" \
  "v_add_f32_dpp v218, v218, v218 quad_perm:[1,0,3,2] row_mask:0xf bank_mask:0xf bound_ctrl:1\n" \
  "s_add_u32 s37, s37, s38\n" \
  "v_add_f32_dpp v216, v216, v216 quad_perm:[2,3,0,1] row_mask:0xf bank_mask:0xf bound_ctrl:1\n" \
  "v_add_f32_dpp v218, v218, v218 quad_perm:[2,3,0,1] row_mask:0xf bank_mask:0xf bound_ctrl:1\n" \
  "s_mul_i32 s36, s36, 0x4c957f2d\n" \
  "v_add_f32_dpp v216, v216, v216 row_half_mirror row_mask:0xf bank_mask:0xf bound_ctrl:1\n" \
  "v_add_f32_dpp v218, v218, v218 row_half_mirror row_mask:0xf bank_mask:0xf bound_ctrl:1\n" \
  "s_add_u32 s56, s36, 0xf767814f\n" \
  "v_add_f32_dpp v216, v216, v216 row_mirror row_mask:0xf bank_mask:0xf bound_ctrl:1\n" \
  "v_add_f32_dpp v218, v218, v218 row_mirror row_mask:0xf bank_mask:0xf bound_ctrl:1\n" \
  "s_addc_u32 s57, s37, 0x14057b7e\n" \
  "v_mov_b32 v217, v216\n" \
  "v_mov_b32 v219, v218\n" \
  "v_mbcnt_lo_u32_b32 v220, -1, 0\n" \
  "v_mbcnt_hi_u32_b32 v220, -1, v220\n" \
  "v_permlane16_swap_b32 v216, v217\n" \
  "v_permlane16_swap_b32 v218, v219\n" \
  "v_min_u32 v221, 47, v220\n" \
  "v_lshlrev_b32 v221, 3, v221\n" \
  "v_add_u32 v221, 157312, v221\n" \
  "v_add_f32 v216, v216, v217\n" \
  "v_add_f32 v217, v218, v219\n" \
  "v_mul_f32 v218, 0xbacccccd, v216\n" \
  "v_mul_f32 v217, 0x3acccccd, v217\n" \
  "v_fma_f32 v216, -v218, v218, v217\n" \
  "v_max_f32 v216, 0, v216\n" \
  "v_add_f32 v216, 0x3727c5ac, v216\n" \
  "v_rsq_f32 v216, v216\n" \
  "v_mov_b32 v217, s56\n" \
  "v_mov_b32 v219, s57\n" \
  "v_lshrrev_b32 v220, 1, v220\n" \
  "v_lshlrev_b32 v222, 16, v212\n" \
  "v_and_b32 v223, 0xffff0000, v212\n" \
  "ds_write_b64 v221, v[222:223]\n" \
  "v_mov_b32 v221, 157248\n" \
  "s_mov_b32 exec_lo, 1\n" \
  "s_mov_b32 exec_hi, 1\n" \
  "buffer_store_dwordx4 v[216:219], v220, s[44:47], s58 offen sc1\n" \
  "ds_write2_b32 v221, v217, v219 offset0:0 offset1:1\n" \
  "s_mov_b64 exec, -1\n" \
  "s_waitcnt lgkmcnt(0)\n" \
  "s_mov_b32 s42, 1\n" \
  "s_mov_b32 s41, 7\n" \
  "s_branch 1b\n" \
  "2:\n" \
  "s_barrier\n" \
    \
  "v_mfma_f32_16x16x32_bf16 v[64:67], v[152:155], v[144:147], v[64:67]\n" \
  "ds_read_b128 v[168:171], v166 offset:0\n" \
  "v_mfma_f32_16x16x32_bf16 v[68:71], v[152:155], v[148:151], v[68:71]\n" \
  "ds_read_b128 v[72:75], v166 offset:512\n" \
  "v_mfma_f32_16x16x32_bf16 v[0:3], v[104:107], v[144:147], v[0:3]\n" \
  "ds_read_b128 v[172:175], v166 offset:16\n" \
  "v_mfma_f32_16x16x32_bf16 v[32:35], v[104:107], v[148:151], v[32:35]\n" \
  "ds_read_b128 v[76:79], v166 offset:528\n" \
  "v_mfma_f32_16x16x32_bf16 v[4:7], v[108:111], v[144:147], v[4:7]\n" \
  "ds_read_b128 v[176:179], v166 offset:128\n" \
  "v_mfma_f32_16x16x32_bf16 v[36:39], v[108:111], v[148:151], v[36:39]\n" \
  "ds_read_b128 v[80:83], v166 offset:640\n" \
  "v_mfma_f32_16x16x32_bf16 v[8:11], v[112:115], v[144:147], v[8:11]\n" \
  "ds_read_b128 v[180:183], v166 offset:144\n" \
  "v_mfma_f32_16x16x32_bf16 v[40:43], v[112:115], v[148:151], v[40:43]\n" \
  "ds_read_b128 v[84:87], v166 offset:656\n" \
  "v_mfma_f32_16x16x32_bf16 v[12:15], v[116:119], v[144:147], v[12:15]\n" \
  "ds_read_b128 v[184:187], v166 offset:256\n" \
  "v_mfma_f32_16x16x32_bf16 v[44:47], v[116:119], v[148:151], v[44:47]\n" \
  "ds_read_b128 v[88:91], v166 offset:768\n" \
  "v_mfma_f32_16x16x32_bf16 v[16:19], v[120:123], v[144:147], v[16:19]\n" \
  "ds_read_b128 v[188:191], v166 offset:272\n" \
  "v_mfma_f32_16x16x32_bf16 v[48:51], v[120:123], v[148:151], v[48:51]\n" \
  "ds_read_b128 v[92:95], v166 offset:784\n" \
  "v_mfma_f32_16x16x32_bf16 v[20:23], v[124:127], v[144:147], v[20:23]\n" \
  "ds_read_b128 v[192:195], v166 offset:384\n" \
  "v_mfma_f32_16x16x32_bf16 v[52:55], v[124:127], v[148:151], v[52:55]\n" \
  "ds_read_b128 v[96:99], v166 offset:896\n" \
  "v_mfma_f32_16x16x32_bf16 v[24:27], v[128:131], v[144:147], v[24:27]\n" \
  "ds_read_b128 v[196:199], v166 offset:400\n" \
  "v_mfma_f32_16x16x32_bf16 v[56:59], v[128:131], v[148:151], v[56:59]\n" \
  "ds_read_b128 v[100:103], v166 offset:912\n" \
  "v_mfma_f32_16x16x32_bf16 v[28:31], v[132:135], v[144:147], v[28:31]\n" \
  "v_mfma_f32_16x16x32_bf16 v[60:63], v[132:135], v[148:151], v[60:63]\n" \
  "s_waitcnt lgkmcnt(0)\n" \
  "v_pk_fma_f32 v[0:1], v[64:65], v[168:169], v[0:1]\n" \
  "v_pk_fma_f32 v[2:3], v[64:65], v[170:171], v[2:3]\n" \
  "v_pk_fma_f32 v[4:5], v[64:65], v[172:173], v[4:5]\n" \
  "v_pk_fma_f32 v[6:7], v[64:65], v[174:175], v[6:7]\n" \
  "v_pk_mul_f32 v[0:1], v[0:1], v[72:73]\n" \
  "v_pk_mul_f32 v[2:3], v[2:3], v[74:75]\n" \
  "v_pk_mul_f32 v[4:5], v[4:5], v[76:77]\n" \
  "v_pk_mul_f32 v[6:7], v[6:7], v[78:79]\n" \
  "v_cvt_pk_bf16_f32 v104, v0, v1\n" \
  "v_cvt_pk_bf16_f32 v105, v2, v3\n" \
  "v_cvt_pk_bf16_f32 v106, v4, v5\n" \
  "v_cvt_pk_bf16_f32 v107, v6, v7\n" \
  "buffer_store_dwordx4 v[104:107], v164, s[44:47], s48 offen offset:0 sc1\n" \
  "v_pk_fma_f32 v[32:33], v[68:69], v[168:169], v[32:33]\n" \
  "v_pk_fma_f32 v[34:35], v[68:69], v[170:171], v[34:35]\n" \
  "v_pk_fma_f32 v[36:37], v[68:69], v[172:173], v[36:37]\n" \
  "v_pk_fma_f32 v[38:39], v[68:69], v[174:175], v[38:39]\n" \
  "v_pk_mul_f32 v[32:33], v[32:33], v[72:73]\n" \
  "v_pk_mul_f32 v[34:35], v[34:35], v[74:75]\n" \
  "v_pk_mul_f32 v[36:37], v[36:37], v[76:77]\n" \
  "v_pk_mul_f32 v[38:39], v[38:39], v[78:79]\n" \
  "v_cvt_pk_bf16_f32 v108, v32, v33\n" \
  "v_cvt_pk_bf16_f32 v109, v34, v35\n" \
  "v_cvt_pk_bf16_f32 v110, v36, v37\n" \
  "v_cvt_pk_bf16_f32 v111, v38, v39\n" \
  "buffer_store_dwordx4 v[108:111], v165, s[44:47], s48 offen offset:0 sc1\n" \
  "v_pk_fma_f32 v[8:9], v[64:65], v[176:177], v[8:9]\n" \
  "v_pk_fma_f32 v[10:11], v[64:65], v[178:179], v[10:11]\n" \
  "v_pk_fma_f32 v[12:13], v[64:65], v[180:181], v[12:13]\n" \
  "v_pk_fma_f32 v[14:15], v[64:65], v[182:183], v[14:15]\n" \
  "v_pk_mul_f32 v[8:9], v[8:9], v[80:81]\n" \
  "v_pk_mul_f32 v[10:11], v[10:11], v[82:83]\n" \
  "v_pk_mul_f32 v[12:13], v[12:13], v[84:85]\n" \
  "v_pk_mul_f32 v[14:15], v[14:15], v[86:87]\n" \
  "v_cvt_pk_bf16_f32 v112, v8, v9\n" \
  "v_cvt_pk_bf16_f32 v113, v10, v11\n" \
  "v_cvt_pk_bf16_f32 v114, v12, v13\n" \
  "v_cvt_pk_bf16_f32 v115, v14, v15\n" \
  "buffer_store_dwordx4 v[112:115], v164, s[44:47], s48 offen offset:1280 sc1\n" \
  "v_pk_fma_f32 v[40:41], v[68:69], v[176:177], v[40:41]\n" \
  "v_pk_fma_f32 v[42:43], v[68:69], v[178:179], v[42:43]\n" \
  "v_pk_fma_f32 v[44:45], v[68:69], v[180:181], v[44:45]\n" \
  "v_pk_fma_f32 v[46:47], v[68:69], v[182:183], v[46:47]\n" \
  "v_pk_mul_f32 v[40:41], v[40:41], v[80:81]\n" \
  "v_pk_mul_f32 v[42:43], v[42:43], v[82:83]\n" \
  "v_pk_mul_f32 v[44:45], v[44:45], v[84:85]\n" \
  "v_pk_mul_f32 v[46:47], v[46:47], v[86:87]\n" \
  "v_cvt_pk_bf16_f32 v116, v40, v41\n" \
  "v_cvt_pk_bf16_f32 v117, v42, v43\n" \
  "v_cvt_pk_bf16_f32 v118, v44, v45\n" \
  "v_cvt_pk_bf16_f32 v119, v46, v47\n" \
  "buffer_store_dwordx4 v[116:119], v165, s[44:47], s48 offen offset:1280 sc1\n" \
  "v_pk_fma_f32 v[16:17], v[64:65], v[184:185], v[16:17]\n" \
  "v_pk_fma_f32 v[18:19], v[64:65], v[186:187], v[18:19]\n" \
  "v_pk_fma_f32 v[20:21], v[64:65], v[188:189], v[20:21]\n" \
  "v_pk_fma_f32 v[22:23], v[64:65], v[190:191], v[22:23]\n" \
  "v_pk_mul_f32 v[16:17], v[16:17], v[88:89]\n" \
  "v_pk_mul_f32 v[18:19], v[18:19], v[90:91]\n" \
  "v_pk_mul_f32 v[20:21], v[20:21], v[92:93]\n" \
  "v_pk_mul_f32 v[22:23], v[22:23], v[94:95]\n" \
  "v_cvt_pk_bf16_f32 v120, v16, v17\n" \
  "v_cvt_pk_bf16_f32 v121, v18, v19\n" \
  "v_cvt_pk_bf16_f32 v122, v20, v21\n" \
  "v_cvt_pk_bf16_f32 v123, v22, v23\n" \
  "buffer_store_dwordx4 v[120:123], v164, s[44:47], s49 offen offset:0 sc1\n" \
  "v_pk_fma_f32 v[48:49], v[68:69], v[184:185], v[48:49]\n" \
  "v_pk_fma_f32 v[50:51], v[68:69], v[186:187], v[50:51]\n" \
  "v_pk_fma_f32 v[52:53], v[68:69], v[188:189], v[52:53]\n" \
  "v_pk_fma_f32 v[54:55], v[68:69], v[190:191], v[54:55]\n" \
  "v_pk_mul_f32 v[48:49], v[48:49], v[88:89]\n" \
  "v_pk_mul_f32 v[50:51], v[50:51], v[90:91]\n" \
  "v_pk_mul_f32 v[52:53], v[52:53], v[92:93]\n" \
  "v_pk_mul_f32 v[54:55], v[54:55], v[94:95]\n" \
  "v_cvt_pk_bf16_f32 v124, v48, v49\n" \
  "v_cvt_pk_bf16_f32 v125, v50, v51\n" \
  "v_cvt_pk_bf16_f32 v126, v52, v53\n" \
  "v_cvt_pk_bf16_f32 v127, v54, v55\n" \
  "buffer_store_dwordx4 v[124:127], v165, s[44:47], s49 offen offset:0 sc1\n" \
  "v_pk_fma_f32 v[24:25], v[64:65], v[192:193], v[24:25]\n" \
  "v_pk_fma_f32 v[26:27], v[64:65], v[194:195], v[26:27]\n" \
  "v_pk_fma_f32 v[28:29], v[64:65], v[196:197], v[28:29]\n" \
  "v_pk_fma_f32 v[30:31], v[64:65], v[198:199], v[30:31]\n" \
  "v_pk_mul_f32 v[24:25], v[24:25], v[96:97]\n" \
  "v_pk_mul_f32 v[26:27], v[26:27], v[98:99]\n" \
  "v_pk_mul_f32 v[28:29], v[28:29], v[100:101]\n" \
  "v_pk_mul_f32 v[30:31], v[30:31], v[102:103]\n" \
  "v_cvt_pk_bf16_f32 v128, v24, v25\n" \
  "v_cvt_pk_bf16_f32 v129, v26, v27\n" \
  "v_cvt_pk_bf16_f32 v130, v28, v29\n" \
  "v_cvt_pk_bf16_f32 v131, v30, v31\n" \
  "buffer_store_dwordx4 v[128:131], v164, s[44:47], s49 offen offset:1280 sc1\n" \
  "v_pk_fma_f32 v[56:57], v[68:69], v[192:193], v[56:57]\n" \
  "v_pk_fma_f32 v[58:59], v[68:69], v[194:195], v[58:59]\n" \
  "v_pk_fma_f32 v[60:61], v[68:69], v[196:197], v[60:61]\n" \
  "v_pk_fma_f32 v[62:63], v[68:69], v[198:199], v[62:63]\n" \
  "v_pk_mul_f32 v[56:57], v[56:57], v[96:97]\n" \
  "v_pk_mul_f32 v[58:59], v[58:59], v[98:99]\n" \
  "v_pk_mul_f32 v[60:61], v[60:61], v[100:101]\n" \
  "v_pk_mul_f32 v[62:63], v[62:63], v[102:103]\n" \
  "v_cvt_pk_bf16_f32 v132, v56, v57\n" \
  "v_cvt_pk_bf16_f32 v133, v58, v59\n" \
  "v_cvt_pk_bf16_f32 v134, v60, v61\n" \
  "v_cvt_pk_bf16_f32 v135, v62, v63\n" \
  "buffer_store_dwordx4 v[132:135], v165, s[44:47], s49 offen offset:1280 sc1\n" \
  "9:\n" \
  "s_barrier\n" \
    \
  "s_add_u32 m0, s69, 110592\n" \
  "s_nop 0\n" \
  "buffer_load_dwordx4 v240, s[64:67], s68 offen offset:0 lds\n" \
  "v_mov_b32 v124, s56\n" \
  "v_mov_b32 v125, s57\n" \
  "s_mov_b32 s48, -1\n" \
  "s_mov_b32 s49, 0\n" \
  "s_mov_b32 s50, 0xfffff\n" \
  "s_mov_b32 s51, 0xfffff\n" \
  "s_mov_b32 s52, 0\n" \
  "s_mov_b32 s53, -1\n" \
  "v_mbcnt_lo_u32_b32 v220, -1, 0\n" \
  "v_mbcnt_hi_u32_b32 v220, -1, v220\n" \
  "v_lshrrev_b32 v221, 5, v220\n" \
  "v_mad_i32_i24 v221, v221, -14, 0\n" \
  "v_and_b32 v120, 28, v220\n" \
  "v_lshl_add_u32 v221, v221, 1, v120\n" \
  "v_add_lshl_u32 v221, v221, v220, 4\n" \
  "v_add_u32 v221, 2560, v221\n" \
  "buffer_load_dwordx4 v241, s[64:67], s68 offen offset:1024 lds\n" \
  "v_and_b32 v120, 31, v220\n" \
  "v_cmp_eq_u32 vcc, 20, v120\n" \
  "v_lshlrev_b32 v128, 3, v220\n" \
  "v_lshlrev_b32 v220, 4, v220\n" \
  "s_mov_b32 s54, 0xc2c80000\n" \
  "v_mov_b32 v122, 0x3f803f80\n" \
  "v_mov_b32 v123, 0\n" \
  "v_cndmask_b32 v120, 0, v122, vcc\n" \
  "v_mov_b32 v121, v120\n" \
  "v_mov_b64 v[144:145], v[122:123]\n" \
  "v_mov_b64 v[146:147], v[122:123]\n" \
  "v_mov_b64 v[148:149], v[120:121]\n" \
  "v_mov_b64 v[150:151], v[120:121]\n" \
  "v_mov_b64 v[152:153], v[120:121]\n" \
  "v_mov_b64 v[154:155], v[120:121]\n" \
  "v_mov_b64 v[156:157], v[120:121]\n" \
  "buffer_load_dwordx4 v242, s[64:67], s68 offen offset:2048 lds\n" \
  "s_add_u32 s68, s68, 196608\n" \
  "v_mov_b64 v[158:159], v[120:121]\n" \
  "v_mov_b64 v[160:161], v[120:121]\n" \
  "v_mov_b64 v[162:163], v[120:121]\n" \
  "v_mov_b64 v[172:173], v[122:123]\n" \
  "v_mov_b64 v[174:175], v[122:123]\n" \
  "v_mov_b64 v[176:177], v[120:121]\n" \
  "v_mov_b64 v[178:179], v[120:121]\n" \
  "v_mov_b64 v[180:181], v[120:121]\n" \
  "v_mov_b64 v[182:183], v[120:121]\n" \
  "v_mov_b64 v[184:185], v[120:121]\n" \
  "v_mov_b64 v[186:187], v[120:121]\n" \
  "v_mov_b64 v[188:189], v[120:121]\n" \
  "v_mov_b64 v[190:191], v[120:121]\n" \
  "v_mov_b64 v[200:201], v[122:123]\n" \
  "v_mov_b64 v[202:203], v[122:123]\n" \
  "v_mov_b64 v[204:205], v[120:121]\n" \
  "s_add_u32 m0, s69, 0\n" \
  "s_nop 0\n" \
  "buffer_load_dwordx4 v240, s[64:67], s68 offen offset:0 lds\n" \
  "v_mov_b64 v[206:207], v[120:121]\n" \
  "v_mov_b64 v[208:209], v[120:121]\n" \
  "v_mov_b64 v[210:211], v[120:121]\n" \
  "v_mov_b64 v[212:213], v[120:121]\n" \
  "v_mov_b64 v[214:215], v[120:121]\n" \
  "v_mov_b64 v[216:217], v[120:121]\n" \
  "v_mov_b64 v[218:219], v[120:121]\n" \
  "v_mov_b32 v223, 0xdddd\n" \
  "v_mov_b32 v126, 1\n" \
  "v_mov_b64 v[0:1], 0\n" \
  "v_mov_b64 v[2:3], 0\n" \
  "v_mov_b64 v[4:5], 0\n" \
  "v_mov_b64 v[6:7], 0\n" \
  "v_mov_b64 v[8:9], 0\n" \
  "v_mov_b64 v[10:11], 0\n" \
  "v_mov_b64 v[12:13], 0\n" \
  "buffer_load_dwordx4 v241, s[64:67], s68 offen offset:1024 lds\n" \
  "v_mov_b64 v[14:15], 0\n" \
  "v_mov_b64 v[16:17], 0\n" \
  "v_mov_b64 v[18:19], 0\n" \
  "v_mov_b64 v[20:21], 0\n" \
  "v_mov_b64 v[22:23], 0\n" \
  "v_mov_b64 v[24:25], 0\n" \
  "v_mov_b64 v[26:27], 0\n" \
  "v_mov_b64 v[28:29], 0\n" \
  "v_mov_b64 v[30:31], 0\n" \
  "v_mov_b64 v[32:33], 0\n" \
  "v_mov_b64 v[34:35], 0\n" \
  "v_mov_b64 v[36:37], 0\n" \
  "v_mov_b64 v[38:39], 0\n" \
  "v_mov_b64 v[40:41], 0\n" \
  "v_mov_b64 v[42:43], 0\n" \
  "v_mov_b64 v[44:45], 0\n" \
  "buffer_load_dwordx4 v242, s[64:67], s68 offen offset:2048 lds\n" \
  "s_add_u32 s68, s68, 196608\n" \
  "v_mov_b64 v[46:47], 0\n" \
  "v_mov_b64 v[48:49], 0\n" \
  "v_mov_b64 v[50:51], 0\n" \
  "v_mov_b64 v[52:53], 0\n" \
  "v_mov_b64 v[54:55], 0\n" \
  "v_mov_b64 v[56:57], 0\n" \
  "v_mov_b64 v[58:59], 0\n" \
  "v_mov_b64 v[60:61], 0\n" \
  "v_mov_b64 v[62:63], 0\n" \
  "v_mov_b64 v[68:69], 0\n" \
  "v_mov_b64 v[70:71], 0\n" \
  "v_mov_b64 v[80:81], 0\n" \
  "v_mov_b64 v[82:83], 0\n" \
  "s_waitcnt vmcnt(0)\n" \
  "s_mov_b64 exec, 0xffff\n" \
  "buffer_store_dwordx2 v[124:125], v128, s[44:47], s75 offen sc1\n" \
  "s_mov_b64 exec, -1\n" \
  "ds_write_b32 v244, v126\n" \
  "s_add_u32 m0, s69, 65536\n" \
  "s_nop 0\n" \
  "buffer_load_dwordx4 v240, s[64:67], s68 offen offset:0 lds\n" \
  "buffer_load_dwordx4 v241, s[64:67], s68 offen offset:1024 lds\n" \
  "buffer_load_dwordx4 v242, s[64:67], s68 offen offset:2048 lds\n" \
  "s_add_u32 s68, s68, 196608\n" \
  "s_add_u32 m0, s69, 12288\n" \
  "s_nop 0\n" \
  "buffer_load_dwordx4 v240, s[64:67], s68 offen offset:0 lds\n" \
  "buffer_load_dwordx4 v241, s[64:67], s68 offen offset:1024 lds\n" \
  "buffer_load_dwordx4 v242, s[64:67], s68 offen offset:2048 lds\n" \
  "s_add_u32 s68, s68, 196608\n" \
  "s_add_u32 m0, s69, 77824\n" \
  "s_nop 0\n" \
  "buffer_load_dwordx4 v240, s[64:67], s68 offen offset:0 lds\n" \
  "buffer_load_dwordx4 v241, s[64:67], s68 offen offset:1024 lds\n" \
  "buffer_load_dwordx4 v242, s[64:67], s68 offen offset:2048 lds\n" \
  "s_add_u32 s68, s68, 196608\n" \
  "v_add_u32 v243, s80, v243\n" \
  "s_mov_b32 exec_lo, 0xaaaaaaaa\n" \
  "s_mov_b32 exec_hi, 0xaaaaaaaa\n" \
  "v_add_u32 v243, s81, v243\n" \
  "s_mov_b64 exec, s[48:49]\n" \
  "v_add_u32 v243, s71, v243\n" \
  "s_mov_b32 exec_lo, 0\n" \
  "s_mov_b32 exec_hi, 3\n" \
  "v_add_u32 v243, s70, v243\n" \
  "s_mov_b64 exec, -1\n" \
  "v_mov_b32 v127, 2\n" \
  "s_waitcnt vmcnt(0)\n" \
  "ds_write_b32 v244, v127\n" \
    \
  "s_mov_b32 s75, 1048576\n" \
  "7:\n" \
  "buffer_load_dwordx2 v[122:123], v243, s[44:47], 0 offen sc1\n" \
  "s_waitcnt vmcnt(0)\n" \
  "v_cmp_ne_u64 vcc, s[56:57], v[122:123]\n" \
  "s_cbranch_vccz 8f\n" \
  "s_mov_b64 exec, vcc\n" \
  "s_sleep 2\n" \
  "s_sub_u32 s75, s75, 1\n" \
  "s_cbranch_scc0 7b\n" \
  "8:\n" \
  "s_mov_b64 exec, -1\n" \
  "s_setprio 3\n" \
  "buffer_load_dwordx4 v[64:67], v220, s[44:47], s73 offen offset:0 sc1\n" \
  "buffer_load_dwordx4 v[72:75], v220, s[44:47], s73 offen offset:1024 sc1\n" \
  "buffer_load_dwordx4 v[76:79], v220, s[44:47], s74 offen offset:0 sc1\n" \
  "buffer_load_dwordx4 v[84:87], v220, s[44:47], s74 offen offset:1024 sc1\n" \
  "buffer_load_dwordx4 v[192:195], v220, s[76:79], 0 offen offset:0 sc1\n" \
  "buffer_load_dwordx4 v[196:199], v220, s[76:79], 0 offen offset:1024 sc1\n" \
  "s_mov_b64 exec, s[48:49]\n" \
  "buffer_load_dwordx4 v[68:71], v220, s[44:47], s73 offen offset:2048 sc1\n" \
  "buffer_load_dwordx4 v[80:83], v220, s[44:47], s74 offen offset:2048 sc1\n" \
  "buffer_load_dwordx4 v[200:203], v220, s[76:79], 0 offen offset:2048 sc1\n" \
  "s_mov_b64 exec, -1\n" \
  "buffer_load_dwordx4 v[136:139], v220, s[76:79], s72 offen offset:0 sc1\n" \
  "buffer_load_dwordx4 v[140:143], v220, s[76:79], s72 offen offset:1024 sc1\n" \
  "s_mov_b64 exec, s[48:49]\n" \
  "buffer_load_dwordx4 v[144:147], v220, s[76:79], s72 offen offset:2048 sc1\n" \
  "s_mov_b64 exec, s[50:51]\n" \
  "buffer_load_dwordx4 v[148:151], v221, s[76:79], s72 offen offset:0 sc1\n" \
  "buffer_load_dwordx4 v[152:155], v221, s[76:79], s72 offen offset:640 sc1\n" \
  "buffer_load_dwordx4 v[156:159], v221, s[76:79], s72 offen offset:1280 sc1\n" \
  "buffer_load_dwordx4 v[160:163], v221, s[76:79], s72 offen offset:1920 sc1\n" \
  "s_mov_b64 exec, -1\n" \
  "s_add_u32 s72, s72, 5120\n" \
  "buffer_load_dwordx4 v[164:167], v220, s[76:79], s72 offen offset:0 sc1\n" \
  "buffer_load_dwordx4 v[168:171], v220, s[76:79], s72 offen offset:1024 sc1\n" \
  "s_mov_b64 exec, s[48:49]\n" \
  "buffer_load_dwordx4 v[172:175], v220, s[76:79], s72 offen offset:2048 sc1\n" \
  "s_mov_b64 exec, s[50:51]\n" \
  "buffer_load_dwordx4 v[176:179], v221, s[76:79], s72 offen offset:0 sc1\n" \
  "buffer_load_dwordx4 v[180:183], v221, s[76:79], s72 offen offset:640 sc1\n" \
  "buffer_load_dwordx4 v[184:187], v221, s[76:79], s72 offen offset:1280 sc1\n" \
  "buffer_load_dwordx4 v[188:191], v221, s[76:79], s72 offen offset:1920 sc1\n" \
  "s_mov_b64 exec, -1\n" \
  "s_add_u32 s72, s72, 5120\n" \
  "v_mov_b32 v126, 9\n" \
  "ds_write_b32 v244, v126\n" \
  "s_waitcnt vmcnt(14)\n" \
  "v_mov_b64 v[120:121], v[66:67]\n" \
  "v_mov_b64 v[66:67], v[74:75]\n" \
  "v_mov_b64 v[74:75], v[70:71]\n" \
  "v_mov_b64 v[70:71], v[120:121]\n" \
  "v_mov_b64 v[120:121], v[78:79]\n" \
  "v_mov_b64 v[78:79], v[86:87]\n" \
  "v_mov_b64 v[86:87], v[82:83]\n" \
  "v_mov_b64 v[82:83], v[120:121]\n" \
    \
  "s_nop 1\n" \
  "v_mfma_f32_32x32x16_bf16 v[88:103], v[198:201], v[66:69], 0\n" \
  "v_mfma_f32_32x32x8_bf16 v[88:103], v[192:193], v[64:65], v[88:103]\n" \
  "v_mfma_f32_32x32x16_bf16 v[104:119], v[198:201], v[72:75], 0\n" \
  "v_mfma_f32_32x32x8_bf16 v[104:119], v[192:193], v[70:71], v[104:119]\n" \
  "v_mfma_f32_32x32x16_bf16 v[224:239], v[198:201], v[78:81], 0\n" \
  "v_mfma_f32_32x32x8_bf16 v[224:239], v[192:193], v[76:77], v[224:239]\n" \
  "v_mfma_f32_32x32x16_bf16 v[120:135], v[198:201], v[84:87], 0\n" \
  "v_mfma_f32_32x32x8_bf16 v[120:135], v[192:193], v[82:83], v[120:135]\n" \
  "s_mov_b64 exec, s[52:53]\n" \
  "s_nop 4\n" \
  "v_max3_f32 v88, v88, v89, v90\n" \
  "v_max3_f32 v91, v91, v92, v93\n" \
  "v_max3_f32 v88, v88, v94, v95\n" \
  "v_max_f32 v88, v88, v91\n" \
  "v_cvt_pk_bf16_f32 v68, -v88, s54\n" \
  "v_max3_f32 v104, v104, v105, v106\n" \
  "v_max3_f32 v107, v107, v108, v109\n" \
  "v_max3_f32 v104, v104, v110, v111\n" \
  "v_max_f32 v104, v104, v107\n" \
  "v_cvt_pk_bf16_f32 v74, -v104, s54\n" \
  "s_mov_b64 exec, -1\n" \
  "s_waitcnt vmcnt(7)\n" \
  "v_mfma_f32_32x32x16_bf16 v[88:103], v[142:145], v[66:69], 0\n" \
  "v_mfma_f32_32x32x8_bf16 v[88:103], v[136:137], v[64:65], v[88:103]\n" \
  "s_mov_b64 exec, s[52:53]\n" \
  "v_max3_f32 v224, v224, v225, v226\n" \
  "v_max3_f32 v227, v227, v228, v229\n" \
  "v_max3_f32 v224, v224, v230, v231\n" \
  "v_max_f32 v224, v224, v227\n" \
  "v_cvt_pk_bf16_f32 v80, -v224, s54\n" \
  "v_max3_f32 v120, v120, v121, v122\n" \
  "v_max3_f32 v123, v123, v124, v125\n" \
  "v_max3_f32 v120, v120, v126, v127\n" \
  "v_max_f32 v120, v120, v123\n" \
  "v_cvt_pk_bf16_f32 v86, -v120, s54\n" \
  "s_mov_b64 exec, -1\n" \
  "v_mov_b64 v[128:129], 0\n" \
  "v_mov_b64 v[130:131], 0\n" \
  "v_mov_b64 v[132:133], 0\n" \
  "v_mov_b64 v[134:135], 0\n" \
  "s_mov_b32 s75, 5\n" \
  "s_setprio 0\n" \
    \
  "s_branch 2f\n" \
  "1:\n" \
  "v_mfma_f32_32x32x16_bf16 v[88:103], v[170:173], v[72:75], 0\n" \
  "v_exp_f32 v120, v120\n" \
  "v_exp_f32 v121, v121\n" \
  "v_exp_f32 v122, v122\n" \
  "v_exp_f32 v123, v123\n" \
  "v_mfma_f32_32x32x8_bf16 v[88:103], v[164:165], v[70:71], v[88:103]\n" \
  "v_exp_f32 v124, v124\n" \
  "v_exp_f32 v125, v125\n" \
  "v_exp_f32 v126, v126\n" \
  "v_exp_f32 v127, v127\n" \
  "v_smfmac_f32_32x32x32_bf16 v[48:63], v[160:163], v[112:119], v223\n" \
  "buffer_load_dwordx4 v[136:139], v220, s[76:79], s72 offen offset:0 sc1\n" \
  "buffer_load_dwordx4 v[140:143], v220, s[76:79], s72 offen offset:1024 sc1\n" \
  "s_mov_b64 exec, s[48:49]\n" \
  "buffer_load_dwordx4 v[144:147], v220, s[76:79], s72 offen offset:2048 sc1\n" \
  "s_mov_b64 exec, s[50:51]\n" \
  "buffer_load_dwordx4 v[148:151], v221, s[76:79], s72 offen offset:0 sc1\n" \
  "buffer_load_dwordx4 v[152:155], v221, s[76:79], s72 offen offset:640 sc1\n" \
  "buffer_load_dwordx4 v[156:159], v221, s[76:79], s72 offen offset:1280 sc1\n" \
  "buffer_load_dwordx4 v[160:163], v221, s[76:79], s72 offen offset:1920 sc1\n" \
  "s_mov_b64 exec, -1\n" \
  "s_add_u32 s72, s72, 5120\n" \
  "v_exp_f32 v128, v128\n" \
  "v_exp_f32 v129, v129\n" \
  "v_exp_f32 v130, v130\n" \
  "v_exp_f32 v131, v131\n" \
  "v_smfmac_f32_32x32x32_bf16 v[0:15], v[176:179], v[120:127], v223\n" \
  "v_exp_f32 v132, v132\n" \
  "v_exp_f32 v133, v133\n" \
  "v_exp_f32 v134, v134\n" \
  "v_exp_f32 v135, v135\n" \
  "v_mfma_f32_32x32x16_bf16 v[104:119], v[170:173], v[78:81], 0\n" \
  "v_exp_f32 v88, v88\n" \
  "v_exp_f32 v89, v89\n" \
  "v_exp_f32 v90, v90\n" \
  "v_exp_f32 v91, v91\n" \
  "v_mfma_f32_32x32x8_bf16 v[104:119], v[164:165], v[76:77], v[104:119]\n" \
  "v_exp_f32 v92, v92\n" \
  "v_exp_f32 v93, v93\n" \
  "v_exp_f32 v94, v94\n" \
  "v_exp_f32 v95, v95\n" \
  "v_smfmac_f32_32x32x32_bf16 v[0:15], v[180:183], v[128:135], v223\n" \
  "v_exp_f32 v96, v96\n" \
  "v_exp_f32 v97, v97\n" \
  "v_exp_f32 v98, v98\n" \
  "v_exp_f32 v99, v99\n" \
  "v_smfmac_f32_32x32x32_bf16 v[16:31], v[176:179], v[88:95], v223\n" \
  "v_exp_f32 v100, v100\n" \
  "v_exp_f32 v101, v101\n" \
  "v_exp_f32 v102, v102\n" \
  "v_exp_f32 v103, v103\n" \
  "v_mfma_f32_32x32x16_bf16 v[120:135], v[170:173], v[84:87], 0\n" \
  "v_exp_f32 v104, v104\n" \
  "v_exp_f32 v105, v105\n" \
  "v_exp_f32 v106, v106\n" \
  "v_exp_f32 v107, v107\n" \
  "v_mfma_f32_32x32x8_bf16 v[120:135], v[164:165], v[82:83], v[120:135]\n" \
  "v_exp_f32 v108, v108\n" \
  "v_exp_f32 v109, v109\n" \
  "v_exp_f32 v110, v110\n" \
  "v_exp_f32 v111, v111\n" \
  "v_smfmac_f32_32x32x32_bf16 v[16:31], v[180:183], v[96:103], v223\n" \
  "v_exp_f32 v112, v112\n" \
  "v_exp_f32 v113, v113\n" \
  "v_exp_f32 v114, v114\n" \
  "v_exp_f32 v115, v115\n" \
  "v_smfmac_f32_32x32x32_bf16 v[32:47], v[176:179], v[104:111], v223\n" \
  "v_exp_f32 v116, v116\n" \
  "v_exp_f32 v117, v117\n" \
  "v_exp_f32 v118, v118\n" \
  "v_exp_f32 v119, v119\n" \
  "v_mfma_f32_32x32x8_bf16 v[88:103], v[174:175], v[68:69], 0\n" \
  "v_exp_f32 v120, v120\n" \
  "v_exp_f32 v121, v121\n" \
  "v_exp_f32 v122, v122\n" \
  "v_exp_f32 v123, v123\n" \
  "v_mfma_f32_32x32x16_bf16 v[88:103], v[166:169], v[64:67], v[88:103]\n" \
  "v_exp_f32 v124, v124\n" \
  "v_exp_f32 v125, v125\n" \
  "v_exp_f32 v126, v126\n" \
  "v_exp_f32 v127, v127\n" \
  "v_smfmac_f32_32x32x32_bf16 v[32:47], v[180:183], v[112:119], v223\n" \
  "v_exp_f32 v128, v128\n" \
  "v_exp_f32 v129, v129\n" \
  "v_exp_f32 v130, v130\n" \
  "v_exp_f32 v131, v131\n" \
  "v_smfmac_f32_32x32x32_bf16 v[48:63], v[176:179], v[120:127], v223\n" \
  "v_exp_f32 v132, v132\n" \
  "v_exp_f32 v133, v133\n" \
  "v_exp_f32 v134, v134\n" \
  "v_exp_f32 v135, v135\n" \
  "v_mfma_f32_32x32x8_bf16 v[104:119], v[174:175], v[74:75], 0\n" \
  "v_exp_f32 v88, v88\n" \
  "v_exp_f32 v89, v89\n" \
  "v_exp_f32 v90, v90\n" \
  "v_exp_f32 v91, v91\n" \
  "v_mfma_f32_32x32x16_bf16 v[104:119], v[166:169], v[70:73], v[104:119]\n" \
  "v_exp_f32 v92, v92\n" \
  "v_exp_f32 v93, v93\n" \
  "v_exp_f32 v94, v94\n" \
  "v_exp_f32 v95, v95\n" \
  "v_smfmac_f32_32x32x32_bf16 v[48:63], v[180:183], v[128:135], v223\n" \
  "v_exp_f32 v96, v96\n" \
  "v_exp_f32 v97, v97\n" \
  "v_exp_f32 v98, v98\n" \
  "v_exp_f32 v99, v99\n" \
  "v_smfmac_f32_32x32x32_bf16 v[0:15], v[184:187], v[88:95], v223\n" \
  "v_exp_f32 v100, v100\n" \
  "v_exp_f32 v101, v101\n" \
  "v_exp_f32 v102, v102\n" \
  "v_exp_f32 v103, v103\n" \
  "v_mfma_f32_32x32x8_bf16 v[120:135], v[174:175], v[80:81], 0\n" \
  "v_exp_f32 v104, v104\n" \
  "v_exp_f32 v105, v105\n" \
  "v_exp_f32 v106, v106\n" \
  "v_exp_f32 v107, v107\n" \
  "v_mfma_f32_32x32x16_bf16 v[120:135], v[166:169], v[76:79], v[120:135]\n" \
  "v_exp_f32 v108, v108\n" \
  "v_exp_f32 v109, v109\n" \
  "v_exp_f32 v110, v110\n" \
  "v_exp_f32 v111, v111\n" \
  "v_smfmac_f32_32x32x32_bf16 v[0:15], v[188:191], v[96:103], v223\n" \
  "v_exp_f32 v112, v112\n" \
  "v_exp_f32 v113, v113\n" \
  "v_exp_f32 v114, v114\n" \
  "v_exp_f32 v115, v115\n" \
  "v_smfmac_f32_32x32x32_bf16 v[16:31], v[184:187], v[104:111], v223\n" \
  "v_exp_f32 v116, v116\n" \
  "v_exp_f32 v117, v117\n" \
  "v_exp_f32 v118, v118\n" \
  "v_exp_f32 v119, v119\n" \
  "v_mfma_f32_32x32x8_bf16 v[88:103], v[174:175], v[86:87], 0\n" \
  "v_exp_f32 v120, v120\n" \
  "v_exp_f32 v121, v121\n" \
  "v_exp_f32 v122, v122\n" \
  "v_exp_f32 v123, v123\n" \
  "v_mfma_f32_32x32x16_bf16 v[88:103], v[166:169], v[82:85], v[88:103]\n" \
  "v_exp_f32 v124, v124\n" \
  "v_exp_f32 v125, v125\n" \
  "v_exp_f32 v126, v126\n" \
  "v_exp_f32 v127, v127\n" \
  "v_smfmac_f32_32x32x32_bf16 v[16:31], v[188:191], v[112:119], v223\n" \
  "v_exp_f32 v128, v128\n" \
  "v_exp_f32 v129, v129\n" \
  "v_exp_f32 v130, v130\n" \
  "v_exp_f32 v131, v131\n" \
  "v_smfmac_f32_32x32x32_bf16 v[32:47], v[184:187], v[120:127], v223\n" \
  "v_exp_f32 v132, v132\n" \
  "v_exp_f32 v133, v133\n" \
  "v_exp_f32 v134, v134\n" \
  "v_exp_f32 v135, v135\n" \
  "s_waitcnt vmcnt(7)\n" \
  "v_mfma_f32_32x32x16_bf16 v[104:119], v[198:201], v[66:69], 0\n" \
  "v_exp_f32 v88, v88\n" \
  "v_exp_f32 v89, v89\n" \
  "v_exp_f32 v90, v90\n" \
  "v_exp_f32 v91, v91\n" \
  "v_mfma_f32_32x32x8_bf16 v[104:119], v[192:193], v[64:65], v[104:119]\n" \
  "v_exp_f32 v92, v92\n" \
  "v_exp_f32 v93, v93\n" \
  "v_exp_f32 v94, v94\n" \
  "v_exp_f32 v95, v95\n" \
  "v_smfmac_f32_32x32x32_bf16 v[32:47], v[188:191], v[128:135], v223\n" \
  "v_exp_f32 v96, v96\n" \
  "v_exp_f32 v97, v97\n" \
  "v_exp_f32 v98, v98\n" \
  "v_exp_f32 v99, v99\n" \
  "v_smfmac_f32_32x32x32_bf16 v[48:63], v[184:187], v[88:95], v223\n" \
  "v_exp_f32 v100, v100\n" \
  "v_exp_f32 v101, v101\n" \
  "v_exp_f32 v102, v102\n" \
  "v_exp_f32 v103, v103\n" \
  "v_mfma_f32_32x32x16_bf16 v[120:135], v[198:201], v[72:75], 0\n" \
  "v_exp_f32 v104, v104\n" \
  "v_exp_f32 v105, v105\n" \
  "v_exp_f32 v106, v106\n" \
  "v_exp_f32 v107, v107\n" \
  "v_mfma_f32_32x32x8_bf16 v[120:135], v[192:193], v[70:71], v[120:135]\n" \
  "v_exp_f32 v108, v108\n" \
  "v_exp_f32 v109, v109\n" \
  "v_exp_f32 v110, v110\n" \
  "v_exp_f32 v111, v111\n" \
  "v_smfmac_f32_32x32x32_bf16 v[48:63], v[188:191], v[96:103], v223\n" \
  "buffer_load_dwordx4 v[164:167], v220, s[76:79], s72 offen offset:0 sc1\n" \
  "buffer_load_dwordx4 v[168:171], v220, s[76:79], s72 offen offset:1024 sc1\n" \
  "s_mov_b64 exec, s[48:49]\n" \
  "buffer_load_dwordx4 v[172:175], v220, s[76:79], s72 offen offset:2048 sc1\n" \
  "s_mov_b64 exec, s[50:51]\n" \
  "buffer_load_dwordx4 v[176:179], v221, s[76:79], s72 offen offset:0 sc1\n" \
  "buffer_load_dwordx4 v[180:183], v221, s[76:79], s72 offen offset:640 sc1\n" \
  "buffer_load_dwordx4 v[184:187], v221, s[76:79], s72 offen offset:1280 sc1\n" \
  "buffer_load_dwordx4 v[188:191], v221, s[76:79], s72 offen offset:1920 sc1\n" \
  "s_mov_b64 exec, -1\n" \
  "s_add_u32 s72, s72, 5120\n" \
  "v_exp_f32 v112, v112\n" \
  "v_exp_f32 v113, v113\n" \
  "v_exp_f32 v114, v114\n" \
  "v_exp_f32 v115, v115\n" \
  "v_smfmac_f32_32x32x32_bf16 v[0:15], v[204:207], v[104:111], v223\n" \
  "v_exp_f32 v116, v116\n" \
  "v_exp_f32 v117, v117\n" \
  "v_exp_f32 v118, v118\n" \
  "v_exp_f32 v119, v119\n" \
  "v_mfma_f32_32x32x16_bf16 v[88:103], v[198:201], v[78:81], 0\n" \
  "v_exp_f32 v120, v120\n" \
  "v_exp_f32 v121, v121\n" \
  "v_exp_f32 v122, v122\n" \
  "v_exp_f32 v123, v123\n" \
  "v_mfma_f32_32x32x8_bf16 v[88:103], v[192:193], v[76:77], v[88:103]\n" \
  "v_exp_f32 v124, v124\n" \
  "v_exp_f32 v125, v125\n" \
  "v_exp_f32 v126, v126\n" \
  "v_exp_f32 v127, v127\n" \
  "v_smfmac_f32_32x32x32_bf16 v[0:15], v[208:211], v[112:119], v223\n" \
  "v_exp_f32 v128, v128\n" \
  "v_exp_f32 v129, v129\n" \
  "v_exp_f32 v130, v130\n" \
  "v_exp_f32 v131, v131\n" \
  "v_smfmac_f32_32x32x32_bf16 v[16:31], v[204:207], v[120:127], v223\n" \
  "v_exp_f32 v132, v132\n" \
  "v_exp_f32 v133, v133\n" \
  "v_exp_f32 v134, v134\n" \
  "v_exp_f32 v135, v135\n" \
  "v_mfma_f32_32x32x16_bf16 v[104:119], v[198:201], v[84:87], 0\n" \
  "v_exp_f32 v88, v88\n" \
  "v_exp_f32 v89, v89\n" \
  "v_exp_f32 v90, v90\n" \
  "v_exp_f32 v91, v91\n" \
  "v_mfma_f32_32x32x8_bf16 v[104:119], v[192:193], v[82:83], v[104:119]\n" \
  "v_exp_f32 v92, v92\n" \
  "v_exp_f32 v93, v93\n" \
  "v_exp_f32 v94, v94\n" \
  "v_exp_f32 v95, v95\n" \
  "v_smfmac_f32_32x32x32_bf16 v[16:31], v[208:211], v[128:135], v223\n" \
  "v_exp_f32 v96, v96\n" \
  "v_exp_f32 v97, v97\n" \
  "v_exp_f32 v98, v98\n" \
  "v_exp_f32 v99, v99\n" \
  "v_smfmac_f32_32x32x32_bf16 v[32:47], v[204:207], v[88:95], v223\n" \
  "v_exp_f32 v100, v100\n" \
  "v_exp_f32 v101, v101\n" \
  "v_exp_f32 v102, v102\n" \
  "v_exp_f32 v103, v103\n" \
  "v_mfma_f32_32x32x8_bf16 v[120:135], v[202:203], v[68:69], 0\n" \
  "v_exp_f32 v104, v104\n" \
  "v_exp_f32 v105, v105\n" \
  "v_exp_f32 v106, v106\n" \
  "v_exp_f32 v107, v107\n" \
  "v_mfma_f32_32x32x16_bf16 v[120:135], v[194:197], v[64:67], v[120:135]\n" \
  "v_exp_f32 v108, v108\n" \
  "v_exp_f32 v109, v109\n" \
  "v_exp_f32 v110, v110\n" \
  "v_exp_f32 v111, v111\n" \
  "v_smfmac_f32_32x32x32_bf16 v[32:47], v[208:211], v[96:103], v223\n" \
  "v_exp_f32 v112, v112\n" \
  "v_exp_f32 v113, v113\n" \
  "v_exp_f32 v114, v114\n" \
  "v_exp_f32 v115, v115\n" \
  "v_smfmac_f32_32x32x32_bf16 v[48:63], v[204:207], v[104:111], v223\n" \
  "v_exp_f32 v116, v116\n" \
  "v_exp_f32 v117, v117\n" \
  "v_exp_f32 v118, v118\n" \
  "v_exp_f32 v119, v119\n" \
  "v_mfma_f32_32x32x8_bf16 v[88:103], v[202:203], v[74:75], 0\n" \
  "v_exp_f32 v120, v120\n" \
  "v_exp_f32 v121, v121\n" \
  "v_exp_f32 v122, v122\n" \
  "v_exp_f32 v123, v123\n" \
  "v_mfma_f32_32x32x16_bf16 v[88:103], v[194:197], v[70:73], v[88:103]\n" \
  "v_exp_f32 v124, v124\n" \
  "v_exp_f32 v125, v125\n" \
  "v_exp_f32 v126, v126\n" \
  "v_exp_f32 v127, v127\n" \
  "v_smfmac_f32_32x32x32_bf16 v[48:63], v[208:211], v[112:119], v223\n" \
  "v_exp_f32 v128, v128\n" \
  "v_exp_f32 v129, v129\n" \
  "v_exp_f32 v130, v130\n" \
  "v_exp_f32 v131, v131\n" \
  "v_smfmac_f32_32x32x32_bf16 v[0:15], v[212:215], v[120:127], v223\n" \
  "v_exp_f32 v132, v132\n" \
  "v_exp_f32 v133, v133\n" \
  "v_exp_f32 v134, v134\n" \
  "v_exp_f32 v135, v135\n" \
  "v_mfma_f32_32x32x8_bf16 v[104:119], v[202:203], v[80:81], 0\n" \
  "v_exp_f32 v88, v88\n" \
  "v_exp_f32 v89, v89\n" \
  "v_exp_f32 v90, v90\n" \
  "v_exp_f32 v91, v91\n" \
  "v_mfma_f32_32x32x16_bf16 v[104:119], v[194:197], v[76:79], v[104:119]\n" \
  "v_exp_f32 v92, v92\n" \
  "v_exp_f32 v93, v93\n" \
  "v_exp_f32 v94, v94\n" \
  "v_exp_f32 v95, v95\n" \
  "v_smfmac_f32_32x32x32_bf16 v[0:15], v[216:219], v[128:135], v223\n" \
  "v_exp_f32 v96, v96\n" \
  "v_exp_f32 v97, v97\n" \
  "v_exp_f32 v98, v98\n" \
  "v_exp_f32 v99, v99\n" \
  "v_smfmac_f32_32x32x32_bf16 v[16:31], v[212:215], v[88:95], v223\n" \
  "v_exp_f32 v100, v100\n" \
  "v_exp_f32 v101, v101\n" \
  "v_exp_f32 v102, v102\n" \
  "v_exp_f32 v103, v103\n" \
  "v_mfma_f32_32x32x8_bf16 v[120:135], v[202:203], v[86:87], 0\n" \
  "v_exp_f32 v104, v104\n" \
  "v_exp_f32 v105, v105\n" \
  "v_exp_f32 v106, v106\n" \
  "v_exp_f32 v107, v107\n" \
  "v_mfma_f32_32x32x16_bf16 v[120:135], v[194:197], v[82:85], v[120:135]\n" \
  "v_exp_f32 v108, v108\n" \
  "v_exp_f32 v109, v109\n" \
  "v_exp_f32 v110, v110\n" \
  "v_exp_f32 v111, v111\n" \
  "v_smfmac_f32_32x32x32_bf16 v[16:31], v[216:219], v[96:103], v223\n" \
  "v_exp_f32 v112, v112\n" \
  "v_exp_f32 v113, v113\n" \
  "v_exp_f32 v114, v114\n" \
  "v_exp_f32 v115, v115\n" \
  "v_smfmac_f32_32x32x32_bf16 v[32:47], v[212:215], v[104:111], v223\n" \
  "v_exp_f32 v116, v116\n" \
  "v_exp_f32 v117, v117\n" \
  "v_exp_f32 v118, v118\n" \
  "v_exp_f32 v119, v119\n" \
  "s_waitcnt vmcnt(7)\n" \
  "v_mfma_f32_32x32x16_bf16 v[88:103], v[142:145], v[66:69], 0\n" \
  "v_exp_f32 v120, v120\n" \
  "v_exp_f32 v121, v121\n" \
  "v_exp_f32 v122, v122\n" \
  "v_exp_f32 v123, v123\n" \
  "v_mfma_f32_32x32x8_bf16 v[88:103], v[136:137], v[64:65], v[88:103]\n" \
  "v_exp_f32 v124, v124\n" \
  "v_exp_f32 v125, v125\n" \
  "v_exp_f32 v126, v126\n" \
  "v_exp_f32 v127, v127\n" \
  "v_smfmac_f32_32x32x32_bf16 v[32:47], v[216:219], v[112:119], v223\n" \
  "v_exp_f32 v128, v128\n" \
  "v_exp_f32 v129, v129\n" \
  "v_exp_f32 v130, v130\n" \
  "v_exp_f32 v131, v131\n" \
  "v_smfmac_f32_32x32x32_bf16 v[48:63], v[212:215], v[120:127], v223\n" \
  "v_exp_f32 v132, v132\n" \
  "v_exp_f32 v133, v133\n" \
  "v_exp_f32 v134, v134\n" \
  "v_exp_f32 v135, v135\n" \
  "2:\n" \
  "v_mfma_f32_32x32x16_bf16 v[104:119], v[142:145], v[72:75], 0\n" \
  "v_exp_f32 v88, v88\n" \
  "v_exp_f32 v89, v89\n" \
  "v_exp_f32 v90, v90\n" \
  "v_exp_f32 v91, v91\n" \
  "v_mfma_f32_32x32x8_bf16 v[104:119], v[136:137], v[70:71], v[104:119]\n" \
  "v_exp_f32 v92, v92\n" \
  "v_exp_f32 v93, v93\n" \
  "v_exp_f32 v94, v94\n" \
  "v_exp_f32 v95, v95\n" \
  "v_smfmac_f32_32x32x32_bf16 v[48:63], v[216:219], v[128:135], v223\n" \
  "buffer_load_dwordx4 v[192:195], v220, s[76:79], s72 offen offset:0 sc1\n" \
  "buffer_load_dwordx4 v[196:199], v220, s[76:79], s72 offen offset:1024 sc1\n" \
  "s_mov_b64 exec, s[48:49]\n" \
  "buffer_load_dwordx4 v[200:203], v220, s[76:79], s72 offen offset:2048 sc1\n" \
  "s_mov_b64 exec, s[50:51]\n" \
  "buffer_load_dwordx4 v[204:207], v221, s[76:79], s72 offen offset:0 sc1\n" \
  "buffer_load_dwordx4 v[208:211], v221, s[76:79], s72 offen offset:640 sc1\n" \
  "buffer_load_dwordx4 v[212:215], v221, s[76:79], s72 offen offset:1280 sc1\n" \
  "buffer_load_dwordx4 v[216:219], v221, s[76:79], s72 offen offset:1920 sc1\n" \
  "s_mov_b64 exec, -1\n" \
  "s_add_u32 s72, s72, 5120\n" \
  "v_exp_f32 v96, v96\n" \
  "v_exp_f32 v97, v97\n" \
  "v_exp_f32 v98, v98\n" \
  "v_exp_f32 v99, v99\n" \
  "v_smfmac_f32_32x32x32_bf16 v[0:15], v[148:151], v[88:95], v223\n" \
  "v_exp_f32 v100, v100\n" \
  "v_exp_f32 v101, v101\n" \
  "v_exp_f32 v102, v102\n" \
  "v_exp_f32 v103, v103\n" \
  "v_mfma_f32_32x32x16_bf16 v[120:135], v[142:145], v[78:81], 0\n" \
  "v_exp_f32 v104, v104\n" \
  "v_exp_f32 v105, v105\n" \
  "v_exp_f32 v106, v106\n" \
  "v_exp_f32 v107, v107\n" \
  "v_mfma_f32_32x32x8_bf16 v[120:135], v[136:137], v[76:77], v[120:135]\n" \
  "v_exp_f32 v108, v108\n" \
  "v_exp_f32 v109, v109\n" \
  "v_exp_f32 v110, v110\n" \
  "v_exp_f32 v111, v111\n" \
  "v_smfmac_f32_32x32x32_bf16 v[0:15], v[152:155], v[96:103], v223\n" \
  "v_exp_f32 v112, v112\n" \
  "v_exp_f32 v113, v113\n" \
  "v_exp_f32 v114, v114\n" \
  "v_exp_f32 v115, v115\n" \
  "v_smfmac_f32_32x32x32_bf16 v[16:31], v[148:151], v[104:111], v223\n" \
  "v_exp_f32 v116, v116\n" \
  "v_exp_f32 v117, v117\n" \
  "v_exp_f32 v118, v118\n" \
  "v_exp_f32 v119, v119\n" \
  "v_mfma_f32_32x32x16_bf16 v[88:103], v[142:145], v[84:87], 0\n" \
  "v_exp_f32 v120, v120\n" \
  "v_exp_f32 v121, v121\n" \
  "v_exp_f32 v122, v122\n" \
  "v_exp_f32 v123, v123\n" \
  "v_mfma_f32_32x32x8_bf16 v[88:103], v[136:137], v[82:83], v[88:103]\n" \
  "v_exp_f32 v124, v124\n" \
  "v_exp_f32 v125, v125\n" \
  "v_exp_f32 v126, v126\n" \
  "v_exp_f32 v127, v127\n" \
  "v_smfmac_f32_32x32x32_bf16 v[16:31], v[152:155], v[112:119], v223\n" \
  "v_exp_f32 v128, v128\n" \
  "v_exp_f32 v129, v129\n" \
  "v_exp_f32 v130, v130\n" \
  "v_exp_f32 v131, v131\n" \
  "v_smfmac_f32_32x32x32_bf16 v[32:47], v[148:151], v[120:127], v223\n" \
  "v_exp_f32 v132, v132\n" \
  "v_exp_f32 v133, v133\n" \
  "v_exp_f32 v134, v134\n" \
  "v_exp_f32 v135, v135\n" \
  "v_mfma_f32_32x32x8_bf16 v[104:119], v[146:147], v[68:69], 0\n" \
  "v_exp_f32 v88, v88\n" \
  "v_exp_f32 v89, v89\n" \
  "v_exp_f32 v90, v90\n" \
  "v_exp_f32 v91, v91\n" \
  "v_mfma_f32_32x32x16_bf16 v[104:119], v[138:141], v[64:67], v[104:119]\n" \
  "v_exp_f32 v92, v92\n" \
  "v_exp_f32 v93, v93\n" \
  "v_exp_f32 v94, v94\n" \
  "v_exp_f32 v95, v95\n" \
  "v_smfmac_f32_32x32x32_bf16 v[32:47], v[152:155], v[128:135], v223\n" \
  "v_exp_f32 v96, v96\n" \
  "v_exp_f32 v97, v97\n" \
  "v_exp_f32 v98, v98\n" \
  "v_exp_f32 v99, v99\n" \
  "v_smfmac_f32_32x32x32_bf16 v[48:63], v[148:151], v[88:95], v223\n" \
  "v_exp_f32 v100, v100\n" \
  "v_exp_f32 v101, v101\n" \
  "v_exp_f32 v102, v102\n" \
  "v_exp_f32 v103, v103\n" \
  "v_mfma_f32_32x32x8_bf16 v[120:135], v[146:147], v[74:75], 0\n" \
  "v_exp_f32 v104, v104\n" \
  "v_exp_f32 v105, v105\n" \
  "v_exp_f32 v106, v106\n" \
  "v_exp_f32 v107, v107\n" \
  "v_mfma_f32_32x32x16_bf16 v[120:135], v[138:141], v[70:73], v[120:135]\n" \
  "v_exp_f32 v108, v108\n" \
  "v_exp_f32 v109, v109\n" \
  "v_exp_f32 v110, v110\n" \
  "v_exp_f32 v111, v111\n" \
  "v_smfmac_f32_32x32x32_bf16 v[48:63], v[152:155], v[96:103], v223\n" \
  "v_exp_f32 v112, v112\n" \
  "v_exp_f32 v113, v113\n" \
  "v_exp_f32 v114, v114\n" \
  "v_exp_f32 v115, v115\n" \
  "v_smfmac_f32_32x32x32_bf16 v[0:15], v[156:159], v[104:111], v223\n" \
  "v_exp_f32 v116, v116\n" \
  "v_exp_f32 v117, v117\n" \
  "v_exp_f32 v118, v118\n" \
  "v_exp_f32 v119, v119\n" \
  "v_mfma_f32_32x32x8_bf16 v[88:103], v[146:147], v[80:81], 0\n" \
  "v_exp_f32 v120, v120\n" \
  "v_exp_f32 v121, v121\n" \
  "v_exp_f32 v122, v122\n" \
  "v_exp_f32 v123, v123\n" \
  "v_mfma_f32_32x32x16_bf16 v[88:103], v[138:141], v[76:79], v[88:103]\n" \
  "v_exp_f32 v124, v124\n" \
  "v_exp_f32 v125, v125\n" \
  "v_exp_f32 v126, v126\n" \
  "v_exp_f32 v127, v127\n" \
  "v_smfmac_f32_32x32x32_bf16 v[0:15], v[160:163], v[112:119], v223\n" \
  "v_exp_f32 v128, v128\n" \
  "v_exp_f32 v129, v129\n" \
  "v_exp_f32 v130, v130\n" \
  "v_exp_f32 v131, v131\n" \
  "v_smfmac_f32_32x32x32_bf16 v[16:31], v[156:159], v[120:127], v223\n" \
  "v_exp_f32 v132, v132\n" \
  "v_exp_f32 v133, v133\n" \
  "v_exp_f32 v134, v134\n" \
  "v_exp_f32 v135, v135\n" \
  "v_mfma_f32_32x32x8_bf16 v[104:119], v[146:147], v[86:87], 0\n" \
  "v_exp_f32 v88, v88\n" \
  "v_exp_f32 v89, v89\n" \
  "v_exp_f32 v90, v90\n" \
  "v_exp_f32 v91, v91\n" \
  "v_mfma_f32_32x32x16_bf16 v[104:119], v[138:141], v[82:85], v[104:119]\n" \
  "v_exp_f32 v92, v92\n" \
  "v_exp_f32 v93, v93\n" \
  "v_exp_f32 v94, v94\n" \
  "v_exp_f32 v95, v95\n" \
  "v_smfmac_f32_32x32x32_bf16 v[16:31], v[160:163], v[128:135], v223\n" \
  "v_exp_f32 v96, v96\n" \
  "v_exp_f32 v97, v97\n" \
  "v_exp_f32 v98, v98\n" \
  "v_exp_f32 v99, v99\n" \
  "v_smfmac_f32_32x32x32_bf16 v[32:47], v[156:159], v[88:95], v223\n" \
  "v_exp_f32 v100, v100\n" \
  "v_exp_f32 v101, v101\n" \
  "v_exp_f32 v102, v102\n" \
  "v_exp_f32 v103, v103\n" \
  "s_waitcnt vmcnt(7)\n" \
  "v_mfma_f32_32x32x16_bf16 v[120:135], v[170:173], v[66:69], 0\n" \
  "v_exp_f32 v104, v104\n" \
  "v_exp_f32 v105, v105\n" \
  "v_exp_f32 v106, v106\n" \
  "v_exp_f32 v107, v107\n" \
  "v_mfma_f32_32x32x8_bf16 v[120:135], v[164:165], v[64:65], v[120:135]\n" \
  "v_exp_f32 v108, v108\n" \
  "v_exp_f32 v109, v109\n" \
  "v_exp_f32 v110, v110\n" \
  "v_exp_f32 v111, v111\n" \
  "v_smfmac_f32_32x32x32_bf16 v[32:47], v[160:163], v[96:103], v223\n" \
  "v_exp_f32 v112, v112\n" \
  "v_exp_f32 v113, v113\n" \
  "v_exp_f32 v114, v114\n" \
  "v_exp_f32 v115, v115\n" \
  "v_smfmac_f32_32x32x32_bf16 v[48:63], v[156:159], v[104:111], v223\n" \
  "s_sub_u32 s75, s75, 1\n" \
  "v_exp_f32 v116, v116\n" \
  "v_exp_f32 v117, v117\n" \
  "v_exp_f32 v118, v118\n" \
  "v_exp_f32 v119, v119\n" \
  "s_cbranch_scc0 1b\n" \
  "s_nop 0\n" \
  "v_smfmac_f32_32x32x32_bf16 v[48:63], v[160:163], v[112:119], v223\n" \
  "s_waitcnt vmcnt(0)\n" \
    \
  "s_mul_i32 s36, s29, 6144\n" \
  "s_add_u32 s36, s36, 131072\n" \
  "s_xor_b32 s38, s29, 1\n" \
  "s_mul_i32 s39, s38, 6144\n" \
  "s_add_u32 s39, s39, 131072\n" \
  "v_add_u32 v64, s36, v220\n" \
  "v_add_u32 v65, s39, v220\n" \
  "v_lshrrev_b32 v66, 1, v220\n" \
  "s_barrier\n" \
  "ds_write_b128 v64, v[32:35] offset:0\n" \
  "ds_write_b128 v64, v[36:39] offset:1024\n" \
  "ds_write_b128 v64, v[40:43] offset:2048\n" \
  "ds_write_b128 v64, v[48:51] offset:3072\n" \
  "ds_write_b128 v64, v[52:55] offset:4096\n" \
  "ds_write_b128 v64, v[56:59] offset:5120\n" \
  "s_waitcnt lgkmcnt(0)\n" \
  "s_barrier\n" \
  "ds_read_b128 v[76:79], v65 offset:2048\n" \
  "ds_read_b128 v[88:91], v65 offset:5120\n" \
  "ds_read_b128 v[68:71], v65 offset:0\n" \
  "ds_read_b128 v[72:75], v65 offset:1024\n" \
  "ds_read_b128 v[80:83], v65 offset:3072\n" \
  "ds_read_b128 v[84:87], v65 offset:4096\n" \
  "s_waitcnt lgkmcnt(4)\n" \
  "v_pk_add_f32 v[8:9], v[8:9], v[76:77]\n" \
  "v_pk_add_f32 v[10:11], v[10:11], v[78:79]\n" \
  "v_pk_add_f32 v[24:25], v[24:25], v[88:89]\n" \
  "v_pk_add_f32 v[26:27], v[26:27], v[90:91]\n" \
  "v_mov_b32 v92, v8\n" \
  "v_mov_b32 v93, v8\n" \
  "v_mov_b32 v94, v24\n" \
  "v_mov_b32 v95, v24\n" \
  "s_waitcnt lgkmcnt(2)\n" \
  "v_permlane32_swap_b32 v92, v93\n" \
  "v_pk_add_f32 v[0:1], v[0:1], v[68:69]\n" \
  "v_pk_add_f32 v[2:3], v[2:3], v[70:71]\n" \
  "v_permlane32_swap_b32 v94, v95\n" \
  "v_rcp_f32 v92, v93\n" \
  "v_pk_add_f32 v[4:5], v[4:5], v[72:73]\n" \
  "v_pk_add_f32 v[6:7], v[6:7], v[74:75]\n" \
  "v_rcp_f32 v94, v95\n" \
  "v_pk_mul_f32 v[68:69], v[0:1], v[92:93] op_sel_hi:[1,0]\n" \
  "v_pk_mul_f32 v[70:71], v[2:3], v[92:93] op_sel_hi:[1,0]\n" \
  "v_pk_mul_f32 v[72:73], v[4:5], v[92:93] op_sel_hi:[1,0]\n" \
  "v_pk_mul_f32 v[74:75], v[6:7], v[92:93] op_sel_hi:[1,0]\n" \
  "v_cvt_pk_bf16_f32 v96, v68, v69\n" \
  "v_cvt_pk_bf16_f32 v97, v70, v71\n" \
  "v_cvt_pk_bf16_f32 v98, v72, v73\n" \
  "v_cvt_pk_bf16_f32 v99, v74, v75\n" \
  "buffer_store_dwordx4 v[96:99], v220, s[44:47], s62 offen sc1\n" \
  "s_waitcnt lgkmcnt(0)\n" \
  "v_pk_add_f32 v[16:17], v[16:17], v[80:81]\n" \
  "v_pk_add_f32 v[18:19], v[18:19], v[82:83]\n" \
  "v_pk_add_f32 v[20:21], v[20:21], v[84:85]\n" \
  "v_pk_add_f32 v[22:23], v[22:23], v[86:87]\n" \
  "v_pk_mul_f32 v[68:69], v[16:17], v[94:95] op_sel_hi:[1,0]\n" \
  "v_pk_mul_f32 v[70:71], v[18:19], v[94:95] op_sel_hi:[1,0]\n" \
  "v_pk_mul_f32 v[72:73], v[20:21], v[94:95] op_sel_hi:[1,0]\n" \
  "v_pk_mul_f32 v[74:75], v[22:23], v[94:95] op_sel_hi:[1,0]\n" \
  "v_cvt_pk_bf16_f32 v100, v68, v69\n" \
  "v_cvt_pk_bf16_f32 v101, v70, v71\n" \
  "v_cvt_pk_bf16_f32 v102, v72, v73\n" \
  "v_cvt_pk_bf16_f32 v103, v74, v75\n" \
  "buffer_store_dwordx4 v[100:103], v220, s[44:47], s62 offen offset:1024 sc1\n" \
  "v_pk_mul_f32 v[76:77], v[8:9], v[92:93] op_sel_hi:[1,0]\n" \
  "v_pk_mul_f32 v[78:79], v[10:11], v[92:93] op_sel_hi:[1,0]\n" \
  "v_cvt_pk_bf16_f32 v104, v76, v77\n" \
  "v_cvt_pk_bf16_f32 v105, v78, v79\n" \
  "v_pk_mul_f32 v[76:77], v[24:25], v[94:95] op_sel_hi:[1,0]\n" \
  "v_pk_mul_f32 v[78:79], v[26:27], v[94:95] op_sel_hi:[1,0]\n" \
  "v_cvt_pk_bf16_f32 v106, v76, v77\n" \
  "v_cvt_pk_bf16_f32 v107, v78, v79\n" \
  "s_nop 0\n" \
  "v_permlane32_swap_b32 v104, v106\n" \
  "v_permlane32_swap_b32 v105, v107\n" \
  "buffer_store_dwordx2 v[104:105], v66, s[44:47], s62 offen offset:2048 sc1\n" \
    \
  "v_max3_f32 v12, |v0|, |v1|, |v2|\n" \
  "v_max3_f32 v12, |v3|, |v4|, v12\n" \
  "v_max3_f32 v12, |v5|, |v6|, v12\n" \
  "v_max_f32 v12, |v7|, v12\n" \
  "v_max3_f32 v13, |v16|, |v17|, |v18|\n" \
  "v_max3_f32 v13, |v19|, |v20|, v13\n" \
  "v_max3_f32 v13, |v21|, |v22|, v13\n" \
  "v_max_f32 v13, |v23|, v13\n" \
  "v_pk_add_f32 v[0:1], v[0:1], v[2:3]\n" \
  "v_pk_add_f32 v[0:1], v[0:1], v[4:5]\n" \
  "v_pk_add_f32 v[0:1], v[0:1], v[6:7]\n" \
  "v_pk_add_f32 v[0:1], v[0:1], v[8:9]\n" \
  "v_pk_add_f32 v[0:1], v[0:1], v[10:11]\n" \
  "v_pk_add_f32 v[0:1], v[0:1], v[16:17]\n" \
  "v_pk_add_f32 v[0:1], v[0:1], v[18:19]\n" \
  "v_pk_add_f32 v[0:1], v[0:1], v[20:21]\n" \
  "v_pk_add_f32 v[0:1], v[0:1], v[22:23]\n" \
  "v_pk_add_f32 v[0:1], v[0:1], v[24:25]\n" \
  "v_pk_add_f32 v[0:1], v[0:1], v[26:27]\n" \
  "v_add_f32 v0, v0, v1\n" \
  "v_min_f32 v1, v93, v95\n" \
  "v_min_f32 v12, v12, v13\n" \
  "v_mov_b32 v2, 0x207\n" \
  "v_cmp_class_f32 s[36:37], v0, v2\n" \
  "v_cmp_nlt_f32 vcc, 0xd000000, v1\n" \
  "s_or_b64 s[58:59], vcc, s[36:37]\n" \
  "v_cmp_nle_f32 vcc, 0x00800000, v12\n" \
  "s_or_b64 s[58:59], vcc, s[58:59]\n" \
  ".Lcode_last:\n" \
  ""
#define MAIN_CLOB \
  "v1", "v2", "v3", "v4", "v5", "v6", "v7", "v8", "v9", "v10", "v11", "v12", "v13", "v14", "v15", "v16", "v17", "v18", "v19", "v20", \
  "v21", "v22", "v23", "v24", "v25", "v26", "v27", "v28", "v29", "v30", "v31", "v32", "v33", "v34", "v35", "v36", "v37", "v38", "v39", "v40", \
  "v41", "v42", "v43", "v44", "v45", "v46", "v47", "v48", "v49", "v50", "v51", "v52", "v53", "v54", "v55", "v56", "v57", "v58", "v59", "v60", \
  "v61", "v62", "v63", "v64", "v65", "v66", "v67", "v68", "v69", "v70", "v71", "v72", "v73", "v74", "v75", "v76", "v77", "v78", "v79", "v80", \
  "v81", "v82", "v83", "v84", "v85", "v86", "v87", "v88", "v89", "v90", "v91", "v92", "v93", "v94", "v95", "v96", "v97", "v98", "v99", "v100", \
  "v101", "v102", "v103", "v104", "v105", "v106", "v107", "v108", "v109", "v110", "v111", "v112", "v113", "v114", "v115", "v116", "v117", "v118", "v119", "v120", \
  "v121", "v122", "v123", "v124", "v125", "v126", "v127", "v128", "v129", "v130", "v131", "v132", "v133", "v134", "v135", "v136", "v137", "v138", "v139", "v140", \
  "v141", "v142", "v143", "v144", "v145", "v146", "v147", "v148", "v149", "v150", "v151", "v152", "v153", "v154", "v155", "v156", "v157", "v158", "v159", "v160", \
  "v161", "v162", "v163", "v164", "v165", "v166", "v167", "v168", "v169", "v170", "v171", "v172", "v173", "v174", "v175", "v176", "v177", "v178", "v179", "v180", \
  "v181", "v182", "v183", "v184", "v185", "v186", "v187", "v188", "v189", "v190", "v191", "v192", "v193", "v194", "v195", "v196", "v197", "v198", "v199", "v200", \
  "v201", "v202", "v203", "v204", "v205", "v206", "v207", "v208", "v209", "v210", "v211", "v212", "v213", "v214", "v215", "v216", "v217", "v218", "v219", "v220", \
  "v221", "v222", "v223", "v224", "v225", "v226", "v227", "v228", "v229", "v230", "v231", "v232", "v233", "v234", "v235", "v236", "v237", "v238", "v239", "v240", \
  "v241", "v242", "v243", "v244", "v245", "v246", "v247", "v248", "v249", "v250", "v251", "v252", "v253", "s8", "s9", "s10", "s11", "s12", "s13", "s14", \
  "s15", "s16", "s17", "s18", "s19", "s20", "s21", "s22", "s23", "s27", "s30", "s31", "s33", "s34", "s35", "s36", "s37", "s38", "s39", "s40", \
  "s41", "s42", "s43", "s44", "s45", "s46", "s47", "s48", "s49", "s50", "s51", "s52", "s53", "s54", "s55", "s64", "s65", "s66", "s67", "s68", \
  "s69", "s70", "s71", "s72", "s73", "s74", "s75", "s76", "s77", "s78", "s79", "s80", "s81", "s82", "s83", "scc", "vcc", "memory"

DEV float asf(unsigned u) { return __builtin_bit_cast(float, u); }
DEV unsigned asu(float f) { return __builtin_bit_cast(unsigned, f); }
DEV unsigned pk(float a, float b) { bf16x2 r = {(__bf16)a, (__bf16)b}; return __builtin_bit_cast(unsigned, r); }
DEV unsigned uni(unsigned v) { return __builtin_amdgcn_readfirstlane(v); }

DEV void attn_exact(__amdgpu_buffer_rsrc_t ws, unsigned qoff, unsigned kvoff, int tile, int lane, float (&o)[12]) {
  const bool low = lane < 32;
  const unsigned lk = lane * 16, d = lane & 31, lv = 2560 + (d < 20 ? (d >> 2) * 128 + (d & 3) * 16 : 0) + (lane >> 5) * 8;
  u32x2 qx = __builtin_amdgcn_raw_buffer_load_b64(ws, lk + 8 * tile, qoff, SC1);
  u32x2 qy = __builtin_amdgcn_raw_buffer_load_b64(ws, lk + 1024 + 8 * (1 - tile), qoff, SC1);
  u32x2 qz = __builtin_amdgcn_raw_buffer_load_b64(ws, d * 16 + 2048 + 8 * tile, qoff, SC1);
  if (!low) qz = u32x2{0, 0};
  const bf16x8 q16 = __builtin_bit_cast(bf16x8, u32x4{qx[0], qx[1], qy[0], qy[1]});
  const s16x4 q8 = __builtin_bit_cast(s16x4, qz);
  float m = -__builtin_inff();
  f32x16 acc = {};
#pragma nounroll
  for (int pass = 0; pass < 2; pass++) {
#pragma nounroll
    for (int kt = 0; kt < 64; kt++) {
      const unsigned rec = kvoff + (kt >> 1) * 5120, b = kt & 1;
      u32x2 kx = __builtin_amdgcn_raw_buffer_load_b64(ws, lk + 8 * b, rec, SC1);
      u32x2 ky = __builtin_amdgcn_raw_buffer_load_b64(ws, lk + 1024 + 8 * (1 - b), rec, SC1);
      u32x2 kz = __builtin_amdgcn_raw_buffer_load_b64(ws, d * 16 + 2048 + 8 * b, rec, SC1);
      if (!low) kz = u32x2{0, 0};
      f32x16 sc = {};
      sc = __builtin_amdgcn_mfma_f32_32x32x16_bf16(__builtin_bit_cast(bf16x8, u32x4{kx[0], kx[1], ky[0], ky[1]}), q16, sc, 0, 0, 0);
      sc = __builtin_amdgcn_mfma_f32_32x32x8bf16_1k(__builtin_bit_cast(s16x4, kz), q8, sc, 0, 0, 0);
      if (pass == 0) {
#pragma unroll
        for (int i = 0; i < 16; i++) m = __builtin_fmaxf(m, sc[i]);
      } else {
#pragma unroll
        for (int st = 0; st < 2; st++) {
          const u32x2 v0 = __builtin_amdgcn_raw_buffer_load_b64(ws, lv + (b * 2 + st) * 640, rec, SC1);
          const u32x2 v1 = __builtin_amdgcn_raw_buffer_load_b64(ws, lv + (b * 2 + st) * 640 + 64, rec, SC1);
          u32x4 vv = {v0[0], v0[1], v1[0], v1[1]};
          if (d >= 20) { unsigned c = d == 20 ? 0x3f803f80u : 0u; vv = u32x4{c, c, c, c}; }
          u32x4 p;
#pragma unroll
          for (int i = 0; i < 4; i++) p[i] = pk(__builtin_amdgcn_exp2f(sc[8 * st + 2 * i] - m), __builtin_amdgcn_exp2f(sc[8 * st + 2 * i + 1] - m));
          acc = __builtin_amdgcn_mfma_f32_32x32x16_bf16(__builtin_bit_cast(bf16x8, vv), __builtin_bit_cast(bf16x8, p), acc, 0, 0, 0);
        }
      }
    }
    if (pass == 0) {
      auto r = __builtin_amdgcn_permlane32_swap(asu(m), asu(m), false, false);
      m = __builtin_fmaxf(asf(r[0]), asf(r[1]));
    }
  }
#pragma unroll
  for (int i = 0; i < 12; i++) o[i] = acc[i];
}

__global__ void __launch_bounds__(512, 2)
fused_block(const u16*, const u16*, const u16*, const u16*, const u16*, const u16*, const u16*, u16*, char*) {
  __shared__ __attribute__((aligned(16))) char smem[163840];
  const unsigned blk = blockIdx.x;
  unsigned tid = threadIdx.x;
  u32x2 wsp, wv; u32x4 r0, r1;
  asm volatile(MAIN_ASM : "+{v0}"(tid), "=&{s[24:25]}"(wsp), "=&{s[28:29]}"(wv), "=&{s[56:59]}"(r0), "=&{s[60:63]}"(r1)
               : "{s[4:5]}"(__builtin_amdgcn_kernarg_segment_ptr()), "{s[6:7]}"(lane_tab), "{s26}"(blk), "s"((unsigned)(uintptr_t)smem) : MAIN_CLOB);
  char* ws = (char*)(uni(wsp[0]) | (u64)uni(wsp[1]) << 32);
  const __amdgpu_buffer_rsrc_t wsr = __builtin_amdgcn_make_buffer_rsrc(ws, 0, W_END, 0x20000);
  const u32x2 tag = {uni(r0[0]), uni(r0[1])};
  const unsigned w = uni(wv[1]), qoff = uni(r1[0]), kvoff = uni(r1[1]), dst = uni(r1[2]), flag = uni(r1[3]);
  const int lane = __builtin_amdgcn_mbcnt_hi(-1, __builtin_amdgcn_mbcnt_lo(-1, 0));
  if (__builtin_expect((uni(r0[2]) | uni(r0[3])) != 0, 0)) {
    asm volatile("s_waitcnt vmcnt(0)" ::: "memory");
#pragma nounroll
    for (int t = 0; t < 2; t++) {
      float o[12];
      attn_exact(wsr, qoff, kvoff, t, lane, o);
      const auto l = __builtin_amdgcn_permlane32_swap(asu(o[8]), asu(o[8]), false, false);
      const float inv = __builtin_amdgcn_rcpf(asf(l[1]));
      __builtin_amdgcn_raw_buffer_store_b128(u32x4{pk(o[0] * inv, o[1] * inv), pk(o[2] * inv, o[3] * inv), pk(o[4] * inv, o[5] * inv), pk(o[6] * inv, o[7] * inv)},
                                             wsr, dst + t * 1024 + lane * 16, 0, SC1);
      if (lane < 32) __builtin_amdgcn_raw_buffer_store_b64(u32x2{pk(o[8] * inv, o[9] * inv), pk(o[10] * inv, o[11] * inv)}, wsr, dst + 2048 + (t * 32 + lane) * 8, 0, SC1);
    }
  }
  asm volatile("s_waitcnt vmcnt(0)" ::: "memory");
  if (lane < 16) __builtin_amdgcn_raw_buffer_store_b64(tag, wsr, flag + lane * 8, 0, SC1);
  if (blk == 0 && w == 0) {
#pragma nounroll
    for (int i = 0; i < SPIN; i++) {
      bool bad = false;
#pragma unroll
      for (int k = 0; k < 4; k++) {
        const u32x2 f = __builtin_amdgcn_raw_buffer_load_b64(wsr, W_FH + ((k * 4 + (lane >> 4)) * 64 + (lane & 15)) * FS, 0, SC1);
        bad |= f[0] != tag[0] || f[1] != tag[1];
      }
      if (__builtin_amdgcn_ballot_w64(bad) == 0) break;
      asm volatile("s_sleep 2" ::: "memory");
    }
    if (lane == 0) __builtin_amdgcn_raw_buffer_store_b64(tag, wsr, W_EPOCH, 0, SC1);
  }
}

extern "C" void kernel_launch(void* const* d_in, const int* in_sizes, int n_in, void* d_out, int out_size, void* d_ws, size_t ws_size, hipStream_t stream) {
  fused_block<<<256, 512, 0, stream>>>((const u16*)d_in[0], (const u16*)d_in[1], (const u16*)d_in[2], (const u16*)d_in[4], (const u16*)d_in[5],
                                       (const u16*)d_in[6], (const u16*)d_in[7], (u16*)d_out, (char*)d_ws);
}
```
